```python
import torch

def _softmax(x, axis=-1):
    return torch.softmax(x, dim=axis)
def _gelu(x):
    return torch.nn.functional.gelu(x, approximate="tanh")
def _silu(x):
    return torch.nn.functional.silu(x)
def _sigmoid(x):
    return torch.sigmoid(x)
def _layernorm(x, axis=-1, eps=1e-5):
    m = x.mean(dim=axis, keepdim=True)
    v = x.var(dim=axis, keepdim=True, unbiased=False)
    return (x - m) / torch.sqrt(v + eps)
def _scatter_add(tgt, idx, src, axis=0):
    return tgt.clone().index_add_(axis, idx, src)
def _segment_sum(src, seg, n):
    out = torch.zeros((n,) + src.shape[1:], dtype=src.dtype)
    return out.index_add_(0, seg, src)
def _pad(x, pw):
    flat = tuple(v for pair in reversed(pw) for v in pair)
    return torch.nn.functional.pad(x, flat)

kernel_name = 'sequential_preLN__softmax_full_mha__glu_gated_reglu__prompt'

def setup_inputs():

    _w = (lambda a: (lambda t: t.to(torch.bfloat16) if t.is_floating_point() else t)(torch.as_tensor(a)))
    def _A_setup():
        B, S, D = 1, 2048, 448
        return {
            "x": _w((lambda *s: torch.randn(*s, dtype=torch.bfloat16) * 0.05)(B, S, D)),
            "wqkv": _w((lambda *s: torch.randn(*s, dtype=torch.bfloat16) * 0.05)(D, 3 * D)),
            "wo": _w((lambda *s: torch.randn(*s, dtype=torch.bfloat16) * 0.05)(D, D)),
        }
    _A_in = _A_setup()

    def _B_setup():
        B, S, D, F = 1, 2048, 448, 1792
        return {
            "x": _w((lambda *s: torch.randn(*s, dtype=torch.bfloat16) * 0.05)(B, S, D)),
            "W_gate": _w((lambda *s: torch.randn(*s, dtype=torch.bfloat16) * 0.4)(D, F) * (2.0 / D) ** 0.5),
            "W_up": _w((lambda *s: torch.randn(*s, dtype=torch.bfloat16) * 0.4)(D, F) * (2.0 / D) ** 0.5),
            "W_down": _w((lambda *s: torch.randn(*s, dtype=torch.bfloat16) * 0.4)(F, D) * (1.0 / F) ** 0.5),
        }
    _B_in = _B_setup()

    return {
        "A_x": _A_in["x"],
        "A_wqkv": _A_in["wqkv"],
        "A_wo": _A_in["wo"],
        "B_x": _B_in["x"],
        "B_W_gate": _B_in["W_gate"],
        "B_W_up": _B_in["W_up"],
        "B_W_down": _B_in["W_down"],
    }

def reference(A_x, A_wqkv, A_wo, B_x, B_W_gate, B_W_up, B_W_down):
    def _A_ref(x, wqkv, wo):
        B, S, D = x.shape
        H = 16; Dh = D // H
        qkv = (lambda x, *d: x.permute(*d))((x @ wqkv).reshape(B, S, 3, H, Dh), 2, 0, 3, 1, 4)
        q, k, v = qkv[0], qkv[1], qkv[2]
        scale = Dh ** -0.5
        scores = torch.einsum("bhqd,bhkd->bhqk", q, k) * scale
        p = _softmax(scores, axis=-1)
        o = torch.einsum("bhqk,bhkd->bhqd", p, v)
        return (lambda x, *d: x.permute(*d))(o, 0, 2, 1, 3).reshape(B, S, D) @ wo

    def _B_ref(x, W_gate, W_up, W_down):
        gate = torch.einsum("bsd,df->bsf", x, W_gate)
        up = torch.einsum("bsd,df->bsf", x, W_up)
        gate = gate * (lambda x, dt: x.to(dt))(gate > 0, gate.dtype)
        h = gate * up
        return torch.einsum("bsf,fd->bsd", h, W_down)

    x = A_x
    h = x + _A_ref(_layernorm(x, axis=-1), A_wqkv, A_wo)
    h2 = h + _B_ref(_layernorm(h, axis=-1), B_W_gate, B_W_up, B_W_down)
    return h2
```

```cpp
#include <hip/hip_runtime.h>
#include <stdint.h>

typedef __attribute__((ext_vector_type(8))) __bf16 bf16x8;
typedef __attribute__((ext_vector_type(2))) __bf16 bf16x2;
typedef __attribute__((ext_vector_type(2))) float f32x2;
typedef __attribute__((ext_vector_type(4))) float f32x4;
typedef __attribute__((ext_vector_type(16))) float f32x16;
typedef __attribute__((ext_vector_type(4))) unsigned u32x4;
typedef __attribute__((ext_vector_type(2))) unsigned u32x2;
typedef __attribute__((ext_vector_type(4))) short s16x4;
typedef unsigned long long u64;
typedef __amdgpu_buffer_rsrc_t rsrc_t;
#define DI __device__ __forceinline__
#define LAS __attribute__((address_space(3)))
#define GAS __attribute__((address_space(1)))
#define RLX_AGENT __ATOMIC_RELAXED, __HIP_MEMORY_SCOPE_AGENT
#define SC1 16

constexpr unsigned WS_E = 0;
constexpr unsigned WS_FA1 = 4096;
constexpr unsigned WS_FB = WS_FA1 + 32768;
constexpr unsigned WS_FC = WS_FB + 32768;
constexpr unsigned WS_FD = WS_FC + 32768;
constexpr unsigned WS_FA2 = WS_FD + 32768;
constexpr unsigned WS_PROF = 196608;
constexpr unsigned WS_QF = 262144;
constexpr unsigned WS_KV = WS_QF + (2u << 20);
constexpr unsigned WS_OF = WS_KV + (4u << 20);
constexpr unsigned WS_HF = WS_OF + (2u << 20);
constexpr unsigned WS_GF = WS_HF + 64u * 28 * 1024;
constexpr unsigned WS_W4 = WS_GF + 64u * 112 * 1024;
constexpr unsigned WS_WO = WS_W4 + 112u * 28 * 1024;
constexpr unsigned WS_WD = WS_WO + 28u * 16 * 1024;
constexpr unsigned WS_END = WS_WD + 14u * 112 * 1024;

constexpr float QSCALE = 0.18898223650461362f * 1.4426950408889634f;
constexpr float LN_EPS = 1e-5f;

__shared__ __attribute__((aligned(16))) char lds[163840];

#define MFMA32(a, b, c) __builtin_amdgcn_mfma_f32_32x32x16_bf16(__builtin_bit_cast(bf16x8, a), __builtin_bit_cast(bf16x8, b), c, 0, 0, 0)
#define MFMA16(a, b, c) __builtin_amdgcn_mfma_f32_16x16x32_bf16(__builtin_bit_cast(bf16x8, a), __builtin_bit_cast(bf16x8, b), c, 0, 0, 0)
#define SBAR() __builtin_amdgcn_sched_barrier(0)
#define WAITBAR(N) asm volatile("s_waitcnt vmcnt(" #N ") lgkmcnt(0)\n\ts_barrier" ::: "memory")
#define DRAINBAR() do { __builtin_amdgcn_s_waitcnt(0x0070); WAITBAR(0); } while (0)
#define LBAR() asm volatile("s_waitcnt lgkmcnt(0)\n\ts_barrier" ::: "memory")
#define FENCE() asm volatile("" ::: "memory")

DI unsigned rfl(unsigned v) { return __builtin_amdgcn_readfirstlane(v); }
DI unsigned pk2(float lo, float hi) { f32x2 v = {lo, hi}; bf16x2 b = __builtin_convertvector(v, bf16x2); return __builtin_bit_cast(unsigned, b); }
DI float bflo(unsigned v) { return __uint_as_float(v << 16); }
DI float bfhi(unsigned v) { return __uint_as_float(v & 0xffff0000u); }
DI u32x4 ldsr128(unsigned a) { return *(const LAS u32x4*)(size_t)a; }
DI f32x4 ldsrf4(unsigned a) { return *(const LAS f32x4*)(size_t)a; }
DI float ldsrf(unsigned a) { return *(const LAS float*)(size_t)a; }
DI f32x2 ldsrf2(unsigned a) { return *(const LAS f32x2*)(size_t)a; }
DI void ldsw128(unsigned a, u32x4 v) { *(LAS u32x4*)(size_t)a = v; }
DI void ldswf4(unsigned a, f32x4 v) { *(LAS f32x4*)(size_t)a = v; }
DI void ldswf(unsigned a, float v) { *(LAS float*)(size_t)a = v; }
DI void ldswf2(unsigned a, f32x2 v) { *(LAS f32x2*)(size_t)a = v; }
DI u32x2 ldstr(unsigned a) { return __builtin_bit_cast(u32x2, __builtin_amdgcn_ds_read_tr16_b64_v4i16((LAS s16x4*)(size_t)a)); }
DI u32x4 ldstr2(unsigned a, unsigned b) { u32x2 lo = ldstr(a), hi = ldstr(b); return (u32x4){lo[0], lo[1], hi[0], hi[1]}; }
DI float dot2(unsigned a, unsigned b, float c) { return __builtin_amdgcn_fdot2_f32_bf16(__builtin_bit_cast(bf16x2, a), __builtin_bit_cast(bf16x2, b), c, false); }
DI float sum8(u32x4 f, float c) { unsigned a = f[0], b = f[1], d = f[2], e = f[3]; c = dot2(a, 0x3f803f80u, c); c = dot2(b, 0x3f803f80u, c); c = dot2(d, 0x3f803f80u, c); return dot2(e, 0x3f803f80u, c); }
DI float swapadd(float v) { auto r = __builtin_amdgcn_permlane32_swap(__float_as_uint(v), __float_as_uint(v), false, false); return __uint_as_float(r[0]) + __uint_as_float(r[1]); }
DI float swapmax(float v) { auto r = __builtin_amdgcn_permlane32_swap(__float_as_uint(v), __float_as_uint(v), false, false); return fmaxf(__uint_as_float(r[0]), __uint_as_float(r[1])); }
DI float hi2all(float v) { auto r = __builtin_amdgcn_permlane32_swap(__float_as_uint(v), __float_as_uint(v), false, false); return __uint_as_float(r[1]); }
DI void stat8(u32x4 f, float& s1, float& s2) {
#pragma unroll
    for (int j = 0; j < 4; ++j) { const unsigned v = f[j]; const float a = bflo(v), b = bfhi(v); s1 += a; s2 = __builtin_fmaf(a, a, s2); s1 += b; s2 = __builtin_fmaf(b, b, s2); }
}
DI u32x4 pack8(const f32x16& a, int s) { return (u32x4){pk2(a[8 * s], a[8 * s + 1]), pk2(a[8 * s + 2], a[8 * s + 3]), pk2(a[8 * s + 4], a[8 * s + 5]), pk2(a[8 * s + 6], a[8 * s + 7])}; }
DI u32x4 corr_c(float c, bool lo_half) { const unsigned h = pk2(c, c); const float l = c - bfhi(h); return lo_half ? (u32x4){h, pk2(l, 0.f), 0u, 0u} : (u32x4){0u, 0u, 0u, 0u}; }
DI u32x4 corr_m(float m) { const float n = -m; const unsigned h = pk2(n, n); const float l = n - bfhi(h); return (u32x4){pk2(bfhi(h), l), h & 0xffffu, 0u, 0u}; }

DI void dma1(rsrc_t r, unsigned voff, unsigned soff, unsigned m0v) {
    asm volatile("s_mov_b32 m0, %0\n\ts_nop 0\n\tbuffer_load_dwordx4 %1, %2, %3 offen lds" ::"s"(m0v), "v"(voff), "s"(r), "s"(soff) : "memory");
}
DI void dma1c(rsrc_t r, unsigned voff, unsigned soff, unsigned m0v) {
    asm volatile("s_mov_b32 m0, %0\n\ts_nop 0\n\tbuffer_load_dwordx4 %1, %2, %3 offen sc1 lds" ::"s"(m0v), "v"(voff), "s"(r), "s"(soff) : "memory");
}
DI void dma2c(rsrc_t r, unsigned voff, unsigned soff, unsigned m0v) {
    asm volatile("s_mov_b32 m0, %0\n\ts_nop 0\n\tbuffer_load_dwordx4 %1, %2, %3 offen sc1 lds\n\tbuffer_load_dwordx4 %1, %2, %3 offen offset:1024 sc1 lds" ::"s"(m0v), "v"(voff), "s"(r), "s"(soff) : "memory");
}
DI void dma4c(rsrc_t r, unsigned voff, unsigned soff, unsigned m0v) {
    asm volatile("s_mov_b32 m0, %0\n\ts_nop 0\n\tbuffer_load_dwordx4 %1, %2, %3 offen sc1 lds\n\tbuffer_load_dwordx4 %1, %2, %3 offen offset:1024 sc1 lds\n\t"
                 "buffer_load_dwordx4 %1, %2, %3 offen offset:2048 sc1 lds\n\tbuffer_load_dwordx4 %1, %2, %3 offen offset:3072 sc1 lds" ::"s"(m0v), "v"(voff), "s"(r), "s"(soff) : "memory");
}
DI u32x4 ldc(rsrc_t r, unsigned voff, unsigned soff) { return __builtin_amdgcn_raw_buffer_load_b128(r, voff, soff, SC1); }
DI void stc(u32x4 v, rsrc_t r, unsigned voff, unsigned soff) { __builtin_amdgcn_raw_buffer_store_b128(v, r, voff, soff, SC1); }

constexpr unsigned SPIN_CAP = 1u << 20;
DI void publish(char* ws, unsigned off, u64 tag, int lane) { if (lane == 0) __hip_atomic_store((GAS u64*)(ws + off), tag, RLX_AGENT); }
DI void wait16(char* ws, unsigned off, u64 tag, int lane) {
    GAS u64* p = (GAS u64*)(ws + off + (lane & 15) * 128);
#pragma nounroll
    for (unsigned spin = 0; spin < SPIN_CAP; ++spin) {
        const u64 v = __hip_atomic_load(p, RLX_AGENT);
        if (__all(v == tag)) break;
        __builtin_amdgcn_s_sleep(1);
    }
    FENCE();
}

#define STAMP(n)

__global__ __launch_bounds__(512, 2) void block_fwd(const char* __restrict__ x, const char* __restrict__ wqkv, const char* __restrict__ wo, const char* __restrict__ wg,
                                                    const char* __restrict__ wu, const char* __restrict__ wd, char* __restrict__ out, char* ws) {
    STAMP(0);
    const int tid = threadIdx.x, lane = tid & 63;
    const unsigned w = rfl(tid >> 6);
    const unsigned bid = blockIdx.x, xcd = bid & 7, bq = bid >> 3;
    const unsigned t32 = lane & 31, hh = lane >> 5, g4 = lane >> 4, i16 = lane & 15;
    const unsigned LB = (unsigned)(size_t)(LAS char*)lds;
    const rsrc_t rWs = __builtin_amdgcn_make_buffer_rsrc(ws, 0, WS_END, 0x00020000);
    const unsigned l16 = lane * 16;

    u64 tag;
    u32x4 rt[4];
    unsigned rt_type;
    {
        const unsigned i = 4 * (xcd >> 1) + (bq >> 3), h = 8 * (xcd & 1) + (bq & 7);
        const rsrc_t rX = __builtin_amdgcn_make_buffer_rsrc((void*)x, 0, 2048 * 896, 0x00020000);
        const rsrc_t rW = __builtin_amdgcn_make_buffer_rsrc((void*)wqkv, 0, 448 * 2688, 0x00020000);
        constexpr unsigned XB = 0, WB = 114688;
        const unsigned vx0 = (128 * i + 16 * w + (lane >> 3)) * 896 + (((lane & 7) ^ g4) << 4);
        const unsigned vx1 = (128 * i + 16 * w + 8 + (lane >> 3)) * 896 + (((lane & 7) ^ g4 ^ 4) << 4);
        const unsigned vw = (lane >> 2) * 2688 + (lane & 3) * 16;
        const unsigned colb = 56 * h - 8 * (h & 1);
        auto wsoff = [&](unsigned u) { const unsigned c = u / 12, p = (u % 12) >> 2, rg = u & 3; return (64 * c + 16 * rg) * 2688 + p * 896 + colb; };
#pragma unroll
        for (int st = 0; st < 2; ++st) {
#pragma unroll
            for (int c = 2 * st; c < 2 * st + 2; ++c) { dma1(rX, vx0, c * 128, LB + XB + c * 16384 + w * 2048); dma1(rX, vx1, c * 128, LB + XB + c * 16384 + w * 2048 + 1024); }
#pragma unroll
            for (int e = 0; e < 3; ++e) { const unsigned u = 24 * st + 3 * w + e; dma1(rW, vw, wsoff(u), LB + WB + u * 1024); }
        }
#pragma unroll
        for (int c = 4; c < 7; ++c) { dma1(rX, vx0, c * 128, LB + XB + c * 16384 + w * 2048); dma1(rX, vx1, c * 128, LB + XB + c * 16384 + w * 2048 + 1024); }
        u32x4 wl[5];
#pragma unroll
        for (int e = 0; e < 5; ++e) { const unsigned u = min(w + 8 * e, 35u); wl[e] = __builtin_amdgcn_raw_buffer_load_b128(rW, vw, wsoff(48 + u), 0); }
        FENCE();
        {
            const unsigned un = bid;
            rt_type = un < 112 ? 0 : (un < 168 ? 1 : (un < 184 ? 2 : 3));
#pragma unroll
            for (int e = 0; e < 4; ++e) {
                const unsigned v = min(w + 8 * e, 27u);
                const char* base; unsigned off;
                if (rt_type == 0) { const unsigned cb = un >> 2, kq = un & 3; base = v >= 14 ? wu : wg; const unsigned rg = v >= 14 ? v - 14 : v; off = (112 * kq + 8 * rg + (lane >> 3)) * 3584 + cb * 128 + (lane & 7) * 16; }
                else if (rt_type == 1) { const unsigned u2 = un - 112, cb = u2 >> 3, ke = u2 & 7; base = wd; off = (224 * ke + 8 * v + (lane >> 3)) * 896 + cb * 128 + (lane & 7) * 16; }
                else { const unsigned hd = (un - 168) & 15; base = wo; off = min(hd * 25088 + v * 1024 + l16, 448u * 896 - 16); }
                rt[e] = *(const GAS u32x4*)(base + off);
            }
            tag = __hip_atomic_load((GAS u64*)(ws + WS_E), RLX_AGENT) + 1;
        }
        FENCE();
        const unsigned r = w & 3, q4 = i16 >> 2, p4 = i16 & 3;
        unsigned xa[4], wa[4];
#pragma unroll
        for (int s = 0; s < 4; ++s) {
            const unsigned sp = (s + r) & 3;
            xa[s] = LB + XB + (32 * r + t32) * 128 + (((2 * sp + hh) ^ ((t32 >> 1) & 7)) << 4);
            wa[s] = LB + (16 * sp + 8 * hh + q4) * 64 + (((4 * (g4 & 1) + p4 + (h & 1)) & 7) << 3);
        }
        f32x16 a0 = {}, a1 = {};
        float cs0 = 0.f, cs1 = 0.f, s1 = 0.f, s2 = 0.f;
        auto chunkQK = [&](unsigned xo, unsigned wo_) {
#pragma unroll
            for (int s = 0; s < 4; ++s) {
                const u32x4 xf = ldsr128(xa[s] + xo);
                const u32x4 wq = ldstr2(wa[s] + wo_, wa[s] + wo_ + 256), wk = ldstr2(wa[s] + wo_ + 4096, wa[s] + wo_ + 4096 + 256);
                a0 = MFMA32(wq, xf, a0); a1 = MFMA32(wk, xf, a1);
                if (s == 0) { cs0 = sum8(wq, cs0); cs1 = sum8(wk, cs1); }
            }
        };
        auto chunkV = [&](unsigned xo, unsigned wo_) {
#pragma unroll
            for (int s = 0; s < 4; ++s) {
                const u32x4 xf = ldsr128(xa[s] + xo);
                const u32x4 wv = ldstr2(wa[s] + wo_ + 8192, wa[s] + wo_ + 8192 + 256);
                a0 = MFMA32(xf, wv, a0);
                if (s == 0) cs0 = sum8(wv, cs0);
                stat8(xf, s1, s2);
            }
        };
        auto late_w = [&]() {
#pragma unroll
            for (int e = 0; e < 5; ++e) { const unsigned u = min(w + 8 * e, 35u); ldsw128(LB + (u < 24 ? WB + u * 1024 : XB + (u - 24) * 1024) + l16, wl[e]); }
        };
        constexpr unsigned STB = 16384, RSB = STB + 1024, CSB = STB + 2048;
        WAITBAR(23);
        STAMP(1);
        if (w < 4) {
            chunkQK(0, WB); chunkQK(16384, WB + 12288);
            WAITBAR(16);
            chunkQK(2 * 16384, WB + 2 * 12288); chunkQK(3 * 16384, WB + 3 * 12288);
            late_w();
            WAITBAR(5);
            chunkQK(4 * 16384, WB); chunkQK(5 * 16384, WB + 12288); chunkQK(6 * 16384, XB);
            cs0 = swapadd(cs0); cs1 = swapadd(cs1);
            if (hh == 0) { ldswf(LB + CSB + (r * 96 + t32) * 4, cs0); ldswf(LB + CSB + (r * 96 + 32 + t32) * 4, cs1); }
            LBAR();
            float cq = 0.f, ck = 0.f;
#pragma unroll
            for (int e = 0; e < 4; ++e) { cq += ldsrf(LB + CSB + (e * 96 + t32) * 4); ck += ldsrf(LB + CSB + (e * 96 + 32 + t32) * 4); }
            const float mean = ldsrf(LB + STB + (32 * r + t32) * 4), rstd = ldsrf(LB + RSB + (32 * r + t32) * 4);
            const u32x4 mf = corr_m(mean);
            a0 = MFMA32(corr_c(cq, hh == 0), mf, a0); a1 = MFMA32(corr_c(ck, hh == 0), mf, a1);
            const float qs = rstd * QSCALE;
#pragma unroll
            for (int e = 0; e < 16; ++e) { a0[e] *= qs; a1[e] *= rstd; }
            const u32x4 q0 = pack8(a0, 0), k0 = pack8(a1, 0);
            u32x4 q1 = pack8(a0, 1), k1 = pack8(a1, 1);
            if (hh) { q1[2] = 0; q1[3] = 0; k1[2] = 0x3f80u; k1[3] = 0; }
            stc(k0, rWs, l16, WS_KV + (h * 64 + 4 * i + r) * 4096); stc(k1, rWs, l16, WS_KV + (h * 64 + 4 * i + r) * 4096 + 1024);
            stc(q0, rWs, l16, WS_QF + (h * 64 + 4 * i + r) * 2048);
            f32x16 sl = {};
            sl = MFMA32(k0, q0, sl); sl = MFMA32(k1, q1, sl);
            float m = fmaxf(sl[0], sl[1]);
#pragma unroll
            for (int e = 2; e < 16; e += 2) m = fmaxf(fmaxf(m, sl[e]), sl[e + 1]);
            m = swapmax(m);
            if (hh) q1[2] = pk2(-m, 0.f);
            stc(q1, rWs, l16, WS_QF + (h * 64 + 4 * i + r) * 2048 + 1024);
        } else {
            chunkV(0, WB); chunkV(16384, WB + 12288);
            WAITBAR(16);
            chunkV(2 * 16384, WB + 2 * 12288); chunkV(3 * 16384, WB + 3 * 12288);
            late_w();
            WAITBAR(5);
            chunkV(4 * 16384, WB); chunkV(5 * 16384, WB + 12288); chunkV(6 * 16384, XB);
            cs0 = swapadd(cs0); s1 = swapadd(s1); s2 = swapadd(s2);
            const float mean = s1 * (1.f / 448.f), var = fmaxf(s2 * (1.f / 448.f) - mean * mean, 0.f), rstd = __builtin_amdgcn_rsqf(var + LN_EPS);
            if (hh == 0) { ldswf(LB + CSB + (r * 96 + 64 + t32) * 4, cs0); ldswf(LB + STB + (32 * r + t32) * 4, mean); ldswf(LB + RSB + (32 * r + t32) * 4, rstd); }
            LBAR();
            float cv = 0.f;
#pragma unroll
            for (int e = 0; e < 4; ++e) cv += ldsrf(LB + CSB + (e * 96 + 64 + t32) * 4);
            a0 = MFMA32(corr_m(mean), corr_c(cv, hh == 0), a0);
#pragma unroll
            for (int e = 0; e < 4; ++e) { const f32x4 rs = ldsrf4(LB + RSB + (32 * r + 8 * e + 4 * hh) * 4);
#pragma unroll
                for (int j = 0; j < 4; ++j) a0[4 * e + j] *= rs[j]; }
            u32x4 v0 = pack8(a0, 0), v1 = pack8(a0, 1);
            if (t32 >= 28) { const unsigned f = t32 == 28 ? 0x3f803f80u : 0u; v0 = (u32x4){f, f, f, f}; v1 = v0; }
            stc(v0, rWs, l16, WS_KV + (h * 64 + 4 * i + r) * 4096 + 2048); stc(v1, rWs, l16, WS_KV + (h * 64 + 4 * i + r) * 4096 + 3072);
        }
        STAMP(2);
        DRAINBAR();
        if (w == 7) publish(ws, WS_FA1 + (h * 16 + i) * 128, tag, lane);
        STAMP(3);
    }
    {
        const unsigned un = bid, q4 = i16 >> 2, p4 = i16 & 3;
        constexpr unsigned RB = 32768;
#pragma unroll
        for (int e = 0; e < 4; ++e) ldsw128(LB + RB + min(w + 8 * e, 27u) * 1024 + l16, rt[e]);
        LBAR();
        if (rt_type < 3) {
            unsigned la, d2;
            if (rt_type == 0) { la = (g4 & 1) * 14336 + (8 * hh + q4) * 128 + p4 * 8; d2 = 512; }
            else if (rt_type == 1) { la = (4 * hh + q4) * 128 + (g4 & 1) * 32 + p4 * 8; d2 = 1024; }
            else { la = (16 * (g4 >> 1) + 4 * (g4 & 1) + q4) * 896 + p4 * 8; d2 = 7168; }
#pragma unroll
            for (int e = 0; e < 4; ++e) {
                const unsigned f = min(w + 8 * e, 27u);
                unsigned fo, dst;
                if (rt_type == 0) { const unsigned ct = f / 7, ks = f % 7; fo = ks * 2048 + ct * 32; dst = WS_W4 + ((4 * (un >> 2) + ct) * 28 + 7 * (un & 3) + ks) * 1024; }
                else if (rt_type == 1) { const unsigned u2 = un - 112, n = f / 14, ks = f % 14; fo = ks * 2048 + n * 64; dst = WS_WD + ((2 * (u2 >> 3) + n) * 112 + 14 * (u2 & 7) + ks) * 1024; }
                else { fo = f * 32; dst = WS_WO + (f * 16 + (un - 168)) * 1024; }
                u32x4 v = ldstr2(LB + RB + la + fo, LB + RB + la + fo + d2);
                if (rt_type == 2 && g4 == 3) { v[2] = 0; v[3] = 0; }
                stc(v, rWs, l16, dst);
            }
        }
    }
    const unsigned i3 = 2 * xcd + (bq >> 4), j3 = bq & 15;
    u32x4 W[29];
    {
        const unsigned h = 2 * xcd + (bq >> 4), i = bq & 15;
        const unsigned u = w & 3, kh = w >> 2;
        if (w == 7) wait16(ws, WS_FA1 + h * 16 * 128, tag, lane);
        LBAR();
        STAMP(4);
        u32x4 q0, q1;
        asm volatile("buffer_load_dwordx4 %0, %2, %3, %4 offen sc1\n\tbuffer_load_dwordx4 %1, %2, %3, %4 offen offset:1024 sc1"
                     : "=&v"(q0), "=&v"(q1) : "v"(l16), "s"(rWs), "s"(WS_QF + (h * 64 + 4 * i + u) * 2048) : "memory");
        const unsigned kvs = WS_KV + (h * 64 + 32 * kh + u) * 4096;
#pragma unroll
        for (int st = 0; st < 5; ++st) dma4c(rWs, l16, kvs + st * 16384, LB + st * 32768 + w * 4096);
        const unsigned rb = LB + kh * 16384 + l16;
        f32x16 o = {}, sa, sb;
        u32x4 kf0, kf1, vf0, vf1;
        const f32x16 z16 = {};
#define EXP8(S, b) do { _Pragma("unroll") for (int e_ = 0; e_ < 8; ++e_) S[b + e_] = __builtin_amdgcn_exp2f(S[b + e_]); } while (0)
#define STEP(SC, SN, QK, KA, RK, VA, RV) do { \
            if (QK) SN = MFMA32(kf0, q0, z16); \
            EXP8(SC, 0); \
            if (QK) SN = MFMA32(kf1, q1, SN); \
            if (RK) { kf0 = ldsr128(KA); kf1 = ldsr128((KA) + 1024); } \
            const u32x4 p0_ = pack8(SC, 0); \
            EXP8(SC, 8); \
            o = MFMA32(vf0, p0_, o); \
            const u32x4 p1_ = pack8(SC, 1); \
            o = MFMA32(vf1, p1_, o); \
            if (RV) { vf0 = ldsr128((VA) + 2048); vf1 = ldsr128((VA) + 3072); } \
        } while (0)
        asm volatile("s_waitcnt vmcnt(16)\n\ts_barrier" : "+v"(q0), "+v"(q1) :: "memory");
        if (w == 7) publish(ws, WS_FA2 + bid * 8, tag, lane);
        STAMP(5);
        kf0 = ldsr128(rb); kf1 = ldsr128(rb + 1024); vf0 = ldsr128(rb + 2048); vf1 = ldsr128(rb + 3072);
        sa = MFMA32(kf0, q0, z16); sa = MFMA32(kf1, q1, sa);
        kf0 = ldsr128(rb + 4096); kf1 = ldsr128(rb + 4096 + 1024);
        STEP(sa, sb, true, rb + 2 * 4096, true, rb + 4096, true);
        STEP(sb, sa, true, rb + 3 * 4096, true, rb + 2 * 4096, true);
        unsigned prev = rb, slot = 0;
        u32x4 fa0, fa1;
#define STAGE() do { \
            slot = slot == 4 ? 0 : slot + 1; const unsigned cur = rb + slot * 32768; \
            STEP(sa, sb, true, cur, true, prev + 3 * 4096, true); \
            STEP(sb, sa, true, cur + 4096, true, cur, true); \
            STEP(sa, sb, true, cur + 2 * 4096, true, cur + 4096, true); \
            STEP(sb, sa, true, cur + 3 * 4096, true, cur + 2 * 4096, true); \
            prev = cur; } while (0)
#pragma nounroll
        for (unsigned st = 1; st < 4; ++st) {
            if (st == 1) { asm volatile("s_waitcnt vmcnt(12)\n\ts_barrier" ::: "memory"); }
            else { asm volatile("s_waitcnt vmcnt(8)\n\ts_barrier" ::: "memory"); dma4c(rWs, l16, kvs + (st + 3) * 16384, LB + (st - 2) * 32768 + w * 4096); }
            if (st == 3) { fa0 = ldc(rWs, l16, WS_FA2); fa1 = ldc(rWs, l16, WS_FA2 + 1024); FENCE(); }
            STAGE();
        }
        {
            const unsigned tl = (unsigned)tag, th = (unsigned)(tag >> 32);
#pragma nounroll
            for (unsigned spin = 0; spin < SPIN_CAP; ++spin) {
                const bool ok = fa0[0] == tl && fa0[1] == th && fa0[2] == tl && fa0[3] == th && fa1[0] == tl && fa1[1] == th && fa1[2] == tl && fa1[3] == th;
                if (__all(ok)) break;
                __builtin_amdgcn_s_sleep(2);
                FENCE();
                fa0 = ldc(rWs, l16, WS_FA2); fa1 = ldc(rWs, l16, WS_FA2 + 1024);
            }
        }
        asm volatile("s_waitcnt vmcnt(0)\n\ts_barrier" ::: "memory");
        dma4c(rWs, l16, kvs + 7 * 16384, LB + 2 * 32768 + w * 4096);
        {
            const unsigned ct = 7 * j3 + min(w, 6u), wb = WS_W4 + ct * 28 * 1024;
#pragma unroll
            for (int p = 0; p < 28; ++p) W[p] = ldc(rWs, l16, wb + ((p < 14 ? p + 14 * kh : p - 14 * kh) << 10));
        }
        FENCE();
#pragma nounroll
        for (unsigned st = 4; st < 8; ++st) {
            if (st == 5) asm volatile("s_waitcnt vmcnt(28)\n\ts_barrier" ::: "memory");
            STAGE();
        }
        STEP(sa, sb, true, 0u, false, prev + 3 * 4096, true);
        STEP(sb, sa, false, 0u, false, 0u, false);
        STAMP(6);
        constexpr unsigned EB = 3 * 32768;
        {
            const unsigned mine = LB + EB + w * 4096, theirs = LB + EB + (w ^ 4) * 4096;
            f32x4 ka, kb, sa4, sb4;
            if (kh) { sa4 = (f32x4){o[0], o[1], o[2], o[3]}; sb4 = (f32x4){o[4], o[5], o[6], o[7]}; ka = (f32x4){o[8], o[9], o[10], o[11]}; kb = (f32x4){o[12], o[13], o[14], o[15]}; }
            else { ka = (f32x4){o[0], o[1], o[2], o[3]}; kb = (f32x4){o[4], o[5], o[6], o[7]}; sa4 = (f32x4){o[8], o[9], o[10], o[11]}; sb4 = (f32x4){o[12], o[13], o[14], o[15]}; }
            ldswf4(mine + l16, sa4); ldswf4(mine + 1024 + l16, sb4); ldswf(mine + 2048 + lane * 4, o[12]);
            LBAR();
            ka += ldsrf4(theirs + l16); kb += ldsrf4(theirs + 1024 + l16);
            float l = hi2all(o[12] + ldsrf(theirs + 2048 + lane * 4));
            float am = fmaxf(fmaxf(fmaxf(fabsf(ka[0]), fabsf(ka[1])), fmaxf(fabsf(ka[2]), fabsf(ka[3]))), fmaxf(fmaxf(fabsf(kb[0]), fabsf(kb[1])), fmaxf(fabsf(kb[2]), fabsf(kb[3]))));
            const bool good = l >= 0x1p-40f && l <= 0x1p60f && am <= 0x1p120f;
            if (__builtin_expect(!__all(good), 0)) {
                const unsigned kvb = WS_KV + h * 64 * 4096;
                float mx = -INFINITY;
#pragma nounroll
                for (unsigned t = 0; t < 64; ++t) {
                    f32x16 s = MFMA32(ldc(rWs, l16, kvb + t * 4096), q0, z16); s = MFMA32(ldc(rWs, l16, kvb + t * 4096 + 1024), q1, s);
#pragma unroll
                    for (int e = 0; e < 16; ++e) mx = fmaxf(mx, s[e]);
                }
                mx = swapmax(mx);
                o = z16;
#pragma nounroll
                for (unsigned t = 0; t < 64; ++t) {
                    f32x16 s = MFMA32(ldc(rWs, l16, kvb + t * 4096), q0, z16); s = MFMA32(ldc(rWs, l16, kvb + t * 4096 + 1024), q1, s);
#pragma unroll
                    for (int e = 0; e < 16; ++e) s[e] = __builtin_amdgcn_exp2f(s[e] - mx);
                    o = MFMA32(ldc(rWs, l16, kvb + t * 4096 + 2048), pack8(s, 0), o); o = MFMA32(ldc(rWs, l16, kvb + t * 4096 + 3072), pack8(s, 1), o);
                }
                l = hi2all(o[12]);
                if (kh) { ka = (f32x4){o[8], o[9], o[10], o[11]}; kb = (f32x4){o[12], o[13], o[14], o[15]}; }
                else { ka = (f32x4){o[0], o[1], o[2], o[3]}; kb = (f32x4){o[4], o[5], o[6], o[7]}; }
            }
            const float rl = __builtin_amdgcn_rcpf(l);
            u32x4 of = {pk2(ka[0] * rl, ka[1] * rl), pk2(ka[2] * rl, ka[3] * rl), pk2(kb[0] * rl, kb[1] * rl), pk2(kb[2] * rl, kb[3] * rl)};
            if (kh && hh) { of[2] = 0; of[3] = 0; }
            stc(of, rWs, (t32 >> 4) * 16384 + (i16 + 16 * hh) * 16, WS_OF + ((8 * i + 2 * u) * 16 + h) * 1024 + kh * 512);
        }
        DRAINBAR();
        if (w == 7) publish(ws, WS_FB + (i * 16 + h) * 128, tag, lane);
        STAMP(7);
    }
    {
        const unsigned rt32 = 4 * i3 + (j3 >> 2), ct = 7 * (j3 & 3) + min(w, 6u);
        u32x4 wf[16];
        u32x2 xr0, xr1;
        if (w < 7) {
#pragma unroll
            for (int e = 0; e < 16; ++e) wf[e] = ldc(rWs, l16, WS_WO + (ct * 16 + e) * 1024);
            xr0 = *(const GAS u32x2*)(x + (32 * rt32 + i16) * 896 + ct * 32 + g4 * 8);
            xr1 = *(const GAS u32x2*)(x + (32 * rt32 + 16 + i16) * 896 + ct * 32 + g4 * 8);
        } else wait16(ws, WS_FB + i3 * 16 * 128, tag, lane);
        {
            float c = 0.f;
#pragma unroll
            for (int p = 0; p < 28; ++p) c = sum8(W[p], c);
            W[28] = corr_c(swapadd(c), hh == 0);
        }
        LBAR();
        STAMP(8);
        dma4c(rWs, l16, WS_OF + rt32 * 32768 + w * 4096, LB + w * 4096);
        DRAINBAR();
        if (w < 7) {
            f32x4 d0 = {}, d1 = {};
#pragma unroll
            for (int e = 0; e < 16; ++e) { d0 = MFMA16(wf[e], ldsr128(LB + e * 1024 + l16), d0); d1 = MFMA16(wf[e], ldsr128(LB + 16384 + e * 1024 + l16), d1); }
            const u32x2 h0 = {pk2(d0[0] + bflo(xr0[0]), d0[1] + bfhi(xr0[0])), pk2(d0[2] + bflo(xr0[1]), d0[3] + bfhi(xr0[1]))};
            const u32x2 h1 = {pk2(d1[0] + bflo(xr1[0]), d1[1] + bfhi(xr1[0])), pk2(d1[2] + bflo(xr1[1]), d1[3] + bfhi(xr1[1]))};
            const unsigned vo = (i16 + 32 * (g4 >> 1)) * 16 + (g4 & 1) * 8, so = WS_HF + (rt32 * 28 + ct) * 1024;
            __builtin_amdgcn_raw_buffer_store_b64(h0, rWs, vo, so, SC1);
            __builtin_amdgcn_raw_buffer_store_b64(h1, rWs, vo + 256, so, SC1);
        }
        DRAINBAR();
        if (w == 7) { publish(ws, WS_FC + (i3 * 16 + j3) * 128, tag, lane); if (bid == 0) publish(ws, WS_E, tag, lane); wait16(ws, WS_FC + i3 * 16 * 128, tag, lane); }
        STAMP(9);
        LBAR();
        STAMP(10);
    }
    u32x4 Wd[28];
    u32x4 hres;
    const unsigned m5 = j3 / 7, n5 = j3 % 7;
    {
        const unsigned kh = w >> 2;
#pragma unroll
        for (int c = 0; c < 7; ++c) { const unsigned f = (w & 3) * 28 + 14 * kh + 2 * c; dma2c(rWs, l16, WS_HF + (4 * i3 * 28 + f) * 1024, LB + f * 1024); }
        unsigned ha[4];
#pragma unroll
        for (int a = 0; a < 4; ++a) ha[a] = LB + (((a + w) & 3) * 28 + 14 * kh) * 1024 + l16;
        f32x16 acc[4] = {};
        float s1 = 0.f, s2 = 0.f;
        constexpr unsigned SB = 114688;
        const unsigned wdb = WS_WD + ((2 * n5 + (w & 1)) * 112 + 28 * (w >> 1)) * 1024;
#pragma unroll
        for (int p = 0; p < 14; ++p) {
            if (p == 0) WAITBAR(12);
            if (p == 2) WAITBAR(8);
            if (p == 6) WAITBAR(4);
            if (p == 10) DRAINBAR();
#pragma unroll
            for (int a = 0; a < 4; ++a) {
                const u32x4 hf = ldsr128(ha[a] + p * 1024);
                acc[a] = MFMA32(W[p], hf, acc[a]);
                if (a == 0) stat8(hf, s1, s2);
            }
        }
        s1 = swapadd(s1); s2 = swapadd(s2);
        if (hh == 0) ldswf2(LB + SB + (kh * 128 + (w & 3) * 32 + t32) * 8, (f32x2){s1, s2});
        if (j3 < 14) {
#pragma unroll
            for (int p = 0; p < 14; ++p) Wd[p] = ldc(rWs, l16, wdb + p * 1024);
        }
#pragma unroll
        for (int a = 0; a < 4; ++a) ha[a] = kh ? ha[a] - 14336 : ha[a] + 14336;
#pragma unroll
        for (int p = 14; p < 28; ++p) {
            if (p == 16) LBAR();
#pragma unroll
            for (int a = 0; a < 4; ++a) acc[a] = MFMA32(W[p], ldsr128(ha[a] + (p - 14) * 1024), acc[a]);
        }
        if (j3 < 14) {
#pragma unroll
            for (int p = 14; p < 28; ++p) Wd[p] = ldc(rWs, l16, wdb + p * 1024);
            const unsigned row = 64 * m5 + (tid >> 3), c8 = 8 * n5 + (tid & 7);
            hres = ldsr128(LB + ((row >> 5) * 28 + (c8 >> 1)) * 1024 + ((row & 31) + 32 * (c8 & 1)) * 16);
        }
#pragma unroll
        for (int a = 0; a < 4; ++a) {
            const unsigned rtl = (a + w) & 3;
            const f32x2 pa = ldsrf2(LB + SB + (rtl * 32 + t32) * 8), pb = ldsrf2(LB + SB + (128 + rtl * 32 + t32) * 8);
            const float mean = (pa[0] + pb[0]) * (1.f / 448.f), var = fmaxf((pa[1] + pb[1]) * (1.f / 448.f) - mean * mean, 0.f);
            const float r2 = __builtin_amdgcn_rcpf(var + LN_EPS);
            acc[a] = MFMA32(W[28], corr_m(mean), acc[a]);
            float gv[8];
#pragma unroll
            for (int e = 0; e < 8; ++e) gv[e] = fmaxf(acc[a][e] * r2, 0.f) * acc[a][e + 8];
            if (w < 7) stc((u32x4){pk2(gv[0], gv[1]), pk2(gv[2], gv[3]), pk2(gv[4], gv[5]), pk2(gv[6], gv[7])}, rWs, l16, WS_GF + ((4 * i3 + rtl) * 112 + 7 * j3 + w) * 1024);
        }
        STAMP(11);
        DRAINBAR();
        if (w == 7) { publish(ws, WS_FD + (i3 * 16 + j3) * 128, tag, lane); if (j3 < 14) wait16(ws, WS_FD + i3 * 16 * 128, tag, lane); }
        STAMP(12);
    }
    if (j3 < 14) {
        LBAR();
        STAMP(13);
        const unsigned kq = w >> 1;
        const unsigned gs = WS_GF + ((4 * i3 + 2 * m5 + (w & 1)) * 112 + 28 * kq) * 1024;
#pragma unroll
        for (int s = 0; s < 4; ++s) dma4c(rWs, l16, gs + s * 4096, LB + s * 32768 + w * 4096);
        f32x16 acc[2] = {};
        const unsigned ga = LB + kq * 8192 + l16;
#pragma unroll
        for (int s = 0; s < 7; ++s) {
            if (s == 0) WAITBAR(12);
            else if (s < 5) WAITBAR(8);
            else if (s == 5) WAITBAR(4);
            else DRAINBAR();
            if (s >= 1 && s < 4) dma4c(rWs, l16, gs + (s + 3) * 4096, LB + (s - 1) * 32768 + w * 4096);
#pragma unroll
            for (int e = 0; e < 4; ++e) {
                acc[0] = MFMA32(Wd[4 * s + e], ldsr128(ga + (s & 3) * 32768 + e * 1024), acc[0]);
                acc[1] = MFMA32(Wd[4 * s + e], ldsr128(ga + (s & 3) * 32768 + 4096 + e * 1024), acc[1]);
            }
        }
        STAMP(14);
        constexpr unsigned RB = 3 * 32768;
#pragma unroll
        for (int m = 0; m < 2; ++m)
#pragma unroll
            for (int e = 0; e < 4; ++e)
                ldswf4(LB + RB + kq * 16384 + (32 * m + t32) * 256 + (((8 * (w & 1) + 2 * e + hh) ^ (t32 & 15)) << 4), (f32x4){acc[m][4 * e], acc[m][4 * e + 1], acc[m][4 * e + 2], acc[m][4 * e + 3]});
        LBAR();
        const unsigned row = tid >> 3, c8 = tid & 7;
        f32x4 lo = {bflo(hres[0]), bfhi(hres[0]), bflo(hres[1]), bfhi(hres[1])}, hi = {bflo(hres[2]), bfhi(hres[2]), bflo(hres[3]), bfhi(hres[3])};
        f32x4 slo = {}, shi = {};
#pragma unroll
        for (int k = 0; k < 4; ++k) {
            slo += ldsrf4(LB + RB + k * 16384 + row * 256 + (((2 * c8) ^ (row & 15)) << 4));
            shi += ldsrf4(LB + RB + k * 16384 + row * 256 + (((2 * c8 + 1) ^ (row & 15)) << 4));
        }
        lo += slo; hi += shi;
        *(GAS u32x4*)(out + (128 * i3 + 64 * m5 + row) * 896 + n5 * 128 + c8 * 16) = (u32x4){pk2(lo[0], lo[1]), pk2(lo[2], lo[3]), pk2(hi[0], hi[1]), pk2(hi[2], hi[3])};
    }
}

extern "C" void kernel_launch(void* const* d_in, const int* in_sizes, int n_in, void* d_out, int out_size, void* d_ws, size_t ws_size, hipStream_t stream) {
    block_fwd<<<256, 512, 0, stream>>>((const char*)d_in[0], (const char*)d_in[1], (const char*)d_in[2], (const char*)d_in[4], (const char*)d_in[5], (const char*)d_in[6],
                                       (char*)d_out, (char*)d_ws);
}
```

```cpp
#include <hip/hip_runtime.h>
#include <stdint.h>
#include <type_traits>
#include <utility>

typedef __attribute__((ext_vector_type(8))) __bf16 bf16x8;
typedef __attribute__((ext_vector_type(2))) __bf16 bf16x2;
typedef __attribute__((ext_vector_type(2))) float f32x2;
typedef __attribute__((ext_vector_type(4))) float f32x4;
typedef __attribute__((ext_vector_type(16))) float f32x16;
typedef __attribute__((ext_vector_type(4))) unsigned u32x4;
typedef __attribute__((ext_vector_type(2))) unsigned u32x2;
typedef __attribute__((ext_vector_type(4))) short s16x4;
typedef unsigned long long u64;
typedef __amdgpu_buffer_rsrc_t rsrc_t;
#define DI __device__ __forceinline__
#define LAS __attribute__((address_space(3)))
#define GAS __attribute__((address_space(1)))
#define RLX_AGENT __ATOMIC_RELAXED, __HIP_MEMORY_SCOPE_AGENT
#define SC1 16

constexpr unsigned WS_E = 0;
constexpr unsigned WS_FA1 = 4096;
constexpr unsigned WS_FB = WS_FA1 + 32768;
constexpr unsigned WS_FC = WS_FB + 32768;
constexpr unsigned WS_FD = WS_FC + 32768;
constexpr unsigned WS_FA2 = WS_FD + 32768;
constexpr unsigned WS_PROF = 196608;
constexpr unsigned WS_QF = 262144;
constexpr unsigned WS_KV = WS_QF + (2u << 20);
constexpr unsigned WS_OF = WS_KV + (4u << 20);
constexpr unsigned WS_HF = WS_OF + (2u << 20);
constexpr unsigned WS_GF = WS_HF + 64u * 28 * 1024;
constexpr unsigned WS_W4 = WS_GF + 64u * 112 * 1024;
constexpr unsigned WS_WO = WS_W4 + 112u * 29 * 1024;
constexpr unsigned WS_WD = WS_WO + 28u * 16 * 1024;
constexpr unsigned WS_ST = WS_WD + 14u * 112 * 1024;
constexpr unsigned WS_END = WS_ST + 16u * 28 * 1024;
constexpr unsigned OOB = 0x80000000u;

#define KVROT 0
#define ABL_C 5
#define NW4ATT 29
#define P4MAP 0
#define ROT4 0
#define ROT5 n5
constexpr float QSCALE = 0.18898223650461362f * 1.4426950408889634f;
constexpr float LN_EPS = 1e-5f;

__shared__ __attribute__((aligned(16))) char lds[163840];

#define MFMA32(a, b, c) __builtin_amdgcn_mfma_f32_32x32x16_bf16(__builtin_bit_cast(bf16x8, a), __builtin_bit_cast(bf16x8, b), c, 0, 0, 0)
#define MFMA16(a, b, c) __builtin_amdgcn_mfma_f32_16x16x32_bf16(__builtin_bit_cast(bf16x8, a), __builtin_bit_cast(bf16x8, b), c, 0, 0, 0)
#define SBAR() __builtin_amdgcn_sched_barrier(0)
#define WAITBAR(N) asm volatile("s_waitcnt vmcnt(" #N ") lgkmcnt(0)\n\ts_barrier" ::: "memory")
#define DRAINBAR() do { __builtin_amdgcn_s_waitcnt(0x0070); WAITBAR(0); } while (0)
#define LBAR() asm volatile("s_waitcnt lgkmcnt(0)\n\ts_barrier" ::: "memory")
#define BAR() asm volatile("s_barrier" ::: "memory")
#define VBAR(N) asm volatile("s_waitcnt vmcnt(" #N ")\n\ts_barrier" ::: "memory")
#define FENCE() asm volatile("" ::: "memory")

template <int... I, class F> DI void static_for_impl(std::integer_sequence<int, I...>, F&& f) { (f(std::integral_constant<int, I>{}), ...); }
template <int N, class F> DI void static_for(F&& f) { static_for_impl(std::make_integer_sequence<int, N>{}, f); }
#define AI __attribute__((always_inline))
DI unsigned rfl(unsigned v) { return __builtin_amdgcn_readfirstlane(v); }
DI unsigned pk2(float lo, float hi) { f32x2 v = {lo, hi}; bf16x2 b = __builtin_convertvector(v, bf16x2); return __builtin_bit_cast(unsigned, b); }
DI float bflo(unsigned v) { return __uint_as_float(v << 16); }
DI float bfhi(unsigned v) { return __uint_as_float(v & 0xffff0000u); }
DI u32x4 ldsr128(unsigned a) { return *(const LAS u32x4*)(size_t)a; }
DI f32x4 ldsrf4(unsigned a) { return *(const LAS f32x4*)(size_t)a; }
DI float ldsrf(unsigned a) { return *(const LAS float*)(size_t)a; }
DI f32x2 ldsrf2(unsigned a) { return *(const LAS f32x2*)(size_t)a; }
DI void ldsw128(unsigned a, u32x4 v) { *(LAS u32x4*)(size_t)a = v; }
DI void ldswf4(unsigned a, f32x4 v) { *(LAS f32x4*)(size_t)a = v; }
DI void ldswf(unsigned a, float v) { *(LAS float*)(size_t)a = v; }
DI void ldsw64(unsigned a, u32x2 v) { *(LAS u32x2*)(size_t)a = v; }
DI void ldswf2(unsigned a, f32x2 v) { *(LAS f32x2*)(size_t)a = v; }
DI u32x2 ldstr(unsigned a) { return __builtin_bit_cast(u32x2, __builtin_amdgcn_ds_read_tr16_b64_v4i16((LAS s16x4*)(size_t)a)); }
DI u32x4 ldstr2(unsigned a, unsigned b) { u32x2 lo = ldstr(a), hi = ldstr(b); return (u32x4){lo[0], lo[1], hi[0], hi[1]}; }
DI float dot2(unsigned a, unsigned b, float c) { return __builtin_amdgcn_fdot2_f32_bf16(__builtin_bit_cast(bf16x2, a), __builtin_bit_cast(bf16x2, b), c, false); }
DI float sum8(u32x4 f, float c) { unsigned a = f[0], b = f[1], d = f[2], e = f[3]; c = dot2(a, 0x3f803f80u, c); c = dot2(b, 0x3f803f80u, c); c = dot2(d, 0x3f803f80u, c); return dot2(e, 0x3f803f80u, c); }
DI float swapadd(float v) { auto r = __builtin_amdgcn_permlane32_swap(__float_as_uint(v), __float_as_uint(v), false, false); return __uint_as_float(r[0]) + __uint_as_float(r[1]); }
DI float swapmax(float v) { auto r = __builtin_amdgcn_permlane32_swap(__float_as_uint(v), __float_as_uint(v), false, false); return fmaxf(__uint_as_float(r[0]), __uint_as_float(r[1])); }
DI float hi2all(float v) { auto r = __builtin_amdgcn_permlane32_swap(__float_as_uint(v), __float_as_uint(v), false, false); return __uint_as_float(r[1]); }
#define MFMA4(a, b, c) __builtin_amdgcn_mfma_f32_4x4x4bf16_1k(__builtin_bit_cast(s16x4, a), __builtin_bit_cast(s16x4, b), c, 0, 0, 0)
DI void msum(u32x4 f, f32x4& c, f32x4& d) { const u32x2 one = {0x3f803f80u, 0x3f803f80u}, lo = {f[0], f[1]}, hi = {f[2], f[3]}; c = MFMA4(one, lo, c); d = MFMA4(one, hi, d); }
DI void msq(u32x4 f, f32x4& c, f32x4& d) { const u32x2 lo = {f[0], f[1]}, hi = {f[2], f[3]}; c = MFMA4(lo, lo, c); d = MFMA4(hi, hi, d); }
DI float sq8(u32x4 f, float c) { unsigned a = f[0], b = f[1], d = f[2], e = f[3]; c = dot2(a, a, c); c = dot2(b, b, c); c = dot2(d, d, c); return dot2(e, e, c); }
#define PINV(x) asm volatile("" : "+v"(x))
#define PINS(x) do { x = rfl(x); asm volatile("" : "+s"(x)); } while (0)
DI u32x4 pack8(const f32x16& a, int s) { return (u32x4){pk2(a[8 * s], a[8 * s + 1]), pk2(a[8 * s + 2], a[8 * s + 3]), pk2(a[8 * s + 4], a[8 * s + 5]), pk2(a[8 * s + 6], a[8 * s + 7])}; }
DI u32x4 corr_c(float c, bool lo_half) { const unsigned h = pk2(c, c); const float l = c - bfhi(h); return lo_half ? (u32x4){h, pk2(l, 0.f), 0u, 0u} : (u32x4){0u, 0u, 0u, 0u}; }
DI u32x2 split_c(float c) { const unsigned h = pk2(c, c); return (u32x2){h, pk2(c - bfhi(h), 0.f)}; }
DI u32x4 corr_m(float m) { const float n = -m; const unsigned h = pk2(n, n); const float l = n - bfhi(h); return (u32x4){pk2(bfhi(h), l), h & 0xffffu, 0u, 0u}; }

DI void dma1(rsrc_t r, unsigned voff, unsigned soff, unsigned m0v) {
    asm volatile("s_mov_b32 m0, %0\n\ts_nop 0\n\tbuffer_load_dwordx4 %1, %2, %3 offen lds" ::"s"(m0v), "v"(voff), "s"(r), "s"(soff) : "memory");
}
DI void dma4v(rsrc_t r, unsigned v0, unsigned v1, unsigned v2, unsigned v3, unsigned soff, unsigned m0v) {
    asm volatile("s_mov_b32 m0, %0\n\ts_nop 0\n\tbuffer_load_dwordx4 %1, %5, %6 offen lds\n\tbuffer_load_dwordx4 %2, %5, %6 offen offset:1024 lds\n\t"
                 "buffer_load_dwordx4 %3, %5, %6 offen offset:2048 lds\n\tbuffer_load_dwordx4 %4, %5, %6 offen offset:3072 lds" ::"s"(m0v), "v"(v0), "v"(v1), "v"(v2), "v"(v3), "s"(r), "s"(soff) : "memory");
}
DI void dma3v(rsrc_t r, unsigned v0, unsigned v1, unsigned v2, unsigned soff, unsigned m0v) {
    asm volatile("s_mov_b32 m0, %0\n\ts_nop 0\n\tbuffer_load_dwordx4 %1, %4, %5 offen lds\n\tbuffer_load_dwordx4 %2, %4, %5 offen offset:1024 lds\n\t"
                 "buffer_load_dwordx4 %3, %4, %5 offen offset:2048 lds" ::"s"(m0v), "v"(v0), "v"(v1), "v"(v2), "s"(r), "s"(soff) : "memory");
}
DI void dma1c(rsrc_t r, unsigned voff, unsigned soff, unsigned m0v) {
    asm volatile("s_mov_b32 m0, %0\n\ts_nop 0\n\tbuffer_load_dwordx4 %1, %2, %3 offen sc1 lds" ::"s"(m0v), "v"(voff), "s"(r), "s"(soff) : "memory");
}
DI void dma2c(rsrc_t r, unsigned voff, unsigned soff, unsigned m0v) {
    asm volatile("s_mov_b32 m0, %0\n\ts_nop 0\n\tbuffer_load_dwordx4 %1, %2, %3 offen sc1 lds\n\tbuffer_load_dwordx4 %1, %2, %3 offen offset:1024 sc1 lds" ::"s"(m0v), "v"(voff), "s"(r), "s"(soff) : "memory");
}
DI void dma4c(rsrc_t r, unsigned voff, unsigned soff, unsigned m0v) {
    asm volatile("s_mov_b32 m0, %0\n\ts_nop 0\n\tbuffer_load_dwordx4 %1, %2, %3 offen sc1 lds\n\tbuffer_load_dwordx4 %1, %2, %3 offen offset:1024 sc1 lds\n\t"
                 "buffer_load_dwordx4 %1, %2, %3 offen offset:2048 sc1 lds\n\tbuffer_load_dwordx4 %1, %2, %3 offen offset:3072 sc1 lds" ::"s"(m0v), "v"(voff), "s"(r), "s"(soff) : "memory");
}
DI u32x4 ldc(rsrc_t r, unsigned voff, unsigned soff) { return __builtin_amdgcn_raw_buffer_load_b128(r, voff, soff, SC1); }
DI void stc(u32x4 v, rsrc_t r, unsigned voff, unsigned soff) { __builtin_amdgcn_raw_buffer_store_b128(v, r, voff, soff, SC1); }

constexpr unsigned SPIN_CAP = 1u << 20;
DI void publish(char* ws, unsigned off, u64 tag, int lane) { if (lane == 0) __hip_atomic_store((GAS u64*)(ws + off), tag, RLX_AGENT); }
DI void waitf(char* ws, unsigned lane_off, u64 tag) {
    GAS u64* p = (GAS u64*)(ws + lane_off);
#pragma nounroll
    for (unsigned spin = 0; spin < SPIN_CAP; ++spin) {
        const u64 v = __hip_atomic_load(p, RLX_AGENT);
        if (__all(v == tag)) break;
        __builtin_amdgcn_s_sleep(1);
    }
    FENCE();
}

#define STAMP(n)
#define STAMPD(n)

__global__ __launch_bounds__(512, 2) void block_fwd(const char* __restrict__ x, const char* __restrict__ wqkv, const char* __restrict__ wo, const char* __restrict__ wg,
                                                    const char* __restrict__ wu, const char* __restrict__ wd, char* __restrict__ out, char* ws) {
    STAMP(0);
    const int tid = threadIdx.x, lane = tid & 63;
    const unsigned w = rfl(tid >> 6);
    const unsigned bid = blockIdx.x, xcd = bid & 7, bq = bid >> 3;
    const unsigned t32 = lane & 31, hh = lane >> 5, g4 = lane >> 4, i16 = lane & 15;
    const unsigned LB = (unsigned)(size_t)(LAS char*)lds;
    const rsrc_t rWs = __builtin_amdgcn_make_buffer_rsrc(ws, 0, WS_END, 0x00020000);
    const unsigned l16 = lane * 16;
    const unsigned rt_type = bid < 112 ? 0 : (bid < 168 ? 1 : (bid < 184 ? 2 : 3));
    constexpr unsigned RTB = 131072;
    u64 tag = __hip_atomic_load((GAS u64*)(ws + WS_E), RLX_AGENT);
    FENCE();

    {
        const unsigned i = 4 * (xcd >> 1) + (bq >> 3), g = 8 * (xcd & 1) + (bq & 7);
        const unsigned r = w & 3, q4 = i16 >> 2, p4 = i16 & 3;
        constexpr unsigned SLOT = 28672;
        constexpr unsigned MFB = 114688, RSB = MFB + 2048, CSB = RSB + 512;
        f32x16 a0 = {}, a1 = {};
        const bool isv = w < 4 ? g >= 11 : g >= 10;
        const unsigned T0 = 3 * g + (w < 4 ? 0 : 2), rt = 4 * i + r;
        auto dsto = [&](unsigned T) AI { return T < 16 ? WS_QF + (T * 64 + rt) * 2048 : WS_KV + ((T & 15) * 64 + rt) * 4096 + (T >= 32 ? 2048 : 0); };
        unsigned xa[4], wa[4];
        auto rdaddr = [&]() AI {
#pragma unroll
            for (int s = 0; s < 4; ++s) {
                const unsigned sp = (s + r) & 3;
                xa[s] = LB + (32 * r + t32) * 128 + (((2 * sp + hh) ^ ((t32 >> 1) & 7)) << 4);
                wa[s] = LB + 16384 + (16 * sp + 8 * hh + q4) * 192 + ((168 * g) & 15) + (4 * (g4 & 1) + p4) * 8;
            }
        };
        auto fin = [&](auto V, f32x16 a, float rstd, const f32x4* rs, unsigned T, unsigned so) AI {
            u32x4 v0, v1;
            if (V) {
#pragma unroll
                for (int e = 0; e < 16; ++e) a[e] *= rs[e >> 2][e & 3];
                v0 = pack8(a, 0); v1 = pack8(a, 1);
                const unsigned pad = t32 == 28 ? 0x3f803f80u : 0u;
                if (t32 >= 28) { v0 = (u32x4){pad, pad, pad, pad}; v1 = v0; }
            } else {
                const float sc = rstd * (T < 16 ? QSCALE : 1.f);
#pragma unroll
                for (int e = 0; e < 16; ++e) a[e] *= sc;
                v0 = pack8(a, 0); v1 = pack8(a, 1);
                if (hh) { v1[2] = T < 16 ? 0u : 0x3f80u; v1[3] = 0; }
            }
            stc(v0, rWs, l16, so); stc(v1, rWs, l16 + 1024, so);
        };
        if (w < 4) {
            rdaddr();
            const unsigned u2 = bid - 112;
            const char* rbase = rt_type == 0 ? (w >= 2 ? wu : wg) : (rt_type == 1 ? wd : wo);
            const rsrc_t rS = __builtin_amdgcn_make_buffer_rsrc((void*)rbase, 0, rt_type == 3 ? 0 : rt_type == 2 ? 448 * 896 : 448 * 3584, 0x00020000);
            unsigned rstep = rt_type == 0 ? 8 * 3584 : rt_type == 1 ? 8 * 896 : 1024;
            unsigned rvo = (rt_type == 0 ? (lane >> 3) * 3584 + (lane & 7) * 16 : rt_type == 1 ? (lane >> 3) * 896 + (lane & 7) * 16 : l16)
                         + (rt_type == 0 ? 112 * (bid & 3) * 3584 + (bid >> 2) * 128 + (w & 1) * 7 * rstep : (rt_type == 1 ? 224 * (u2 & 7) * 896 + (u2 >> 3) * 128 : ((bid - 168) & 15) * 25088) + w * 7 * rstep);
            unsigned so0 = dsto(T0), so1 = dsto(T0 + 1);
            unsigned cwa = LB + CSB + t32 * 32 + r * 8, cra = LB + CSB + t32 * 32 + hh * 16, mra = LB + MFB + (32 * r + t32) * 16, rra = LB + RSB + (32 * r + 4 * hh) * 4;
            PINV(rvo); PINS(rstep); PINS(so0); PINS(so1); PINV(cwa); PINV(cra); PINV(mra); PINV(rra);
            auto bodyA = [&](auto V) AI {
                f32x4 ca = {}, cb = {}, cc = {}, cd = {};
                u32x4 xf[4], w0[4], w1[4];
                unsigned sb = 0;
                auto rd = [&](int s) AI { xf[s] = ldsr128(xa[s] + sb); w0[s] = ldstr2(wa[s] + sb, wa[s] + sb + 768); w1[s] = ldstr2(wa[s] + sb + 56, wa[s] + sb + 56 + 768); };
                auto mm = [&](int s) AI { if (V) { a0 = MFMA32(xf[s], w0[s], a0); a1 = MFMA32(xf[s], w1[s], a1); } else { a0 = MFMA32(w0[s], xf[s], a0); a1 = MFMA32(w1[s], xf[s], a1); } };
                BAR();
                STAMP(16);
                rd(0); rd(1); rd(2); rd(3); SBAR();
#pragma nounroll
                for (int c = 1; c < 7; ++c) {
                    sb = c == 4 ? 0 : sb + SLOT;
                    BAR(); SBAR();
                    if (c == 5) {
#pragma unroll
                        for (int e = 0; e < 7; ++e) dma1(rS, rvo + e * rstep, 0, LB + RTB + (7 * w + e) * 1024);
                    }
                    msum(w0[0], ca, cb); msum(w1[0], cc, cd); SBAR();
#pragma unroll
                    for (int s = 0; s < 4; ++s) {
                        if (V) a0 = MFMA32(xf[s], w0[s], a0); else a0 = MFMA32(w0[s], xf[s], a0);
                        SBAR();
                        w0[s] = ldstr2(wa[s] + sb, wa[s] + sb + 768); SBAR();
                        if (V) a1 = MFMA32(xf[s], w1[s], a1); else a1 = MFMA32(w1[s], xf[s], a1);
                        SBAR();
                        xf[s] = ldsr128(xa[s] + sb); w1[s] = ldstr2(wa[s] + sb + 56, wa[s] + sb + 56 + 768); SBAR();
                    }
                }
                msum(w0[0], ca, cb); msum(w1[0], cc, cd);
                mm(0); mm(1); mm(2); mm(3); SBAR();
                STAMP(1);
                ldsw64(cwa, split_c(swapadd(ca[0] + cb[0]))); ldsw64(cwa + 1024, split_c(swapadd(cc[0] + cd[0])));
                LBAR();
                STAMP(23);
                const u32x4 c0 = ldsr128(cra), c1 = ldsr128(cra + 1024), mr = ldsr128(mra);
                f32x4 rs[4];
                if (V) {
#pragma unroll
                    for (int e = 0; e < 4; ++e) rs[e] = ldsrf4(rra + 32 * e);
                }
                const u32x4 mf = {mr[0], mr[1], mr[0], mr[1]};
                if (V) { a0 = MFMA32(mf, c0, a0); a1 = MFMA32(mf, c1, a1); } else { a0 = MFMA32(c0, mf, a0); a1 = MFMA32(c1, mf, a1); }
                SBAR();
                fin(V, a0, __uint_as_float(mr[2]), rs, T0, so0);
                fin(V, a1, __uint_as_float(mr[2]), rs, T0 + 1, so1);
            };
            tag += 1; PINV(tag);
            if (isv) bodyA(std::true_type{}); else bodyA(std::false_type{});
            STAMP(2);
            WAITBAR(0);
            STAMP(3);
        } else {
            const rsrc_t rX = __builtin_amdgcn_make_buffer_rsrc((void*)x, 0, 2048 * 896, 0x00020000);
            const rsrc_t rW = __builtin_amdgcn_make_buffer_rsrc((void*)wqkv, 0, 448 * 2688, 0x00020000);
            __builtin_amdgcn_s_setprio(1);
            const unsigned vx0 = (128 * i + 32 * r + (lane >> 3)) * 896 + (((lane & 7) ^ g4) << 4), vx1 = ((vx0 + 8 * 896) ^ 64) - 1024, vx2 = vx0 + 16 * 896 - 2048, vx3 = vx1 + 16 * 896 - 2048;
            unsigned kc = (g + 2 * i) % 7;
            unsigned vw[3];
            STAMP(60);
            dma4v(rX, vx0, vx1, vx2, vx3, kc * 128, LB + r * 4096);
#pragma unroll
            for (int e = 0; e < 3; ++e) { const unsigned f = e * 64 + lane, row = f / 12; vw[e] = (16 * r + row) * 2688 + ((168 * g) & ~15u) + min(f - row * 12, 10u) * 16 - e * 1024; }
            dma3v(rW, vw[0], vw[1], vw[2], kc * 64 * 2688, LB + 16384 + r * 3072);
            STAMP(61);
            kc = kc == 6 ? 0 : kc + 1;
            dma4v(rX, vx0, vx1, vx2, vx3, kc * 128, LB + SLOT + r * 4096);
            dma3v(rW, vw[0], vw[1], vw[2], kc * 64 * 2688, LB + SLOT + 16384 + r * 3072);
            rdaddr();
            unsigned so0 = dsto(T0);
            unsigned cwa = LB + CSB + 2048 + t32 * 32 + r * 8, cra = LB + CSB + 2048 + t32 * 32 + hh * 16, mwa = LB + MFB + (32 * r + t32) * 16, rra = LB + RSB + (32 * r + 4 * hh) * 4, rwa = LB + RSB + (32 * r + t32) * 4;
            PINS(so0); PINV(cwa); PINV(cra); PINV(mwa); PINV(rra); PINV(rwa);
            auto bodyB = [&](auto V) AI {
                f32x4 ca = {}, cb = {}, sa = {}, sb4 = {}, qa = {}, qb = {};
                u32x4 xf[4], w0[4];
                unsigned sb = 0;
                auto rd = [&](int s) AI { xf[s] = ldsr128(xa[s] + sb); w0[s] = ldstr2(wa[s] + sb + 112, wa[s] + sb + 112 + 768); };
                auto mm = [&](int s) AI { if (V) a0 = MFMA32(xf[s], w0[s], a0); else a0 = MFMA32(w0[s], xf[s], a0); };
                auto stt = [&](int s) AI {
                    msum(xf[s], sa, sb4); msq(xf[s], qa, qb);
                };
                kc = kc == 6 ? 0 : kc + 1;
                VBAR(7);
                STAMP(16);
                rd(0); rd(1); rd(2); rd(3); SBAR();
                dma4v(rX, vx0, vx1, vx2, vx3, kc * 128, LB + 2 * SLOT + r * 4096);
                dma3v(rW, vw[0], vw[1], vw[2], kc * 64 * 2688, LB + 2 * SLOT + 16384 + r * 3072);
#pragma nounroll
                for (int c = 1; c < 7; ++c) {
                    sb = c == 4 ? 0 : sb + SLOT;
                    kc = kc == 6 ? 0 : kc + 1;
                    const unsigned nb = LB + ((c + 2) & 3) * SLOT;
                    const rsrc_t rXc = __builtin_amdgcn_make_buffer_rsrc((void*)x, 0, c < ABL_C ? 2048 * 896 : 0, 0x00020000);
                    const rsrc_t rWc = __builtin_amdgcn_make_buffer_rsrc((void*)wqkv, 0, c < ABL_C ? 448 * 2688 : 0, 0x00020000);
                    SBAR();
                    VBAR(7); SBAR();
                    msum(w0[0], ca, cb); stt(0); SBAR(); mm(0); SBAR(); rd(0); SBAR();
                    stt(1); SBAR(); mm(1); SBAR(); rd(1); SBAR();
                    dma4v(rXc, vx0, vx1, vx2, vx3, kc * 128, nb + r * 4096); SBAR();
                    stt(2); SBAR(); mm(2); SBAR(); rd(2); SBAR();
                    stt(3); SBAR(); mm(3); SBAR(); rd(3); SBAR();
                    dma3v(rWc, vw[0], vw[1], vw[2], kc * 64 * 2688, nb + 16384 + r * 3072); SBAR();
                }
                msum(w0[0], ca, cb); stt(0); stt(1); stt(2); stt(3); SBAR();
                mm(0); mm(1); mm(2); mm(3); SBAR();
                STAMP(1);
                qa += qb;
                const float s1 = swapadd(sa[0] + sb4[0]), s2 = swapadd((lane & 2) ? ((lane & 1) ? qa[3] : qa[2]) : ((lane & 1) ? qa[1] : qa[0]));
                const float mean = s1 * (1.f / 448.f), var = fmaxf(s2 * (1.f / 448.f) - mean * mean, 0.f), rstd = __builtin_amdgcn_rsqf(var + LN_EPS);
                u32x4 mf = corr_m(mean);
                ldsw128(mwa, (u32x4){mf[0], mf[1], __float_as_uint(rstd), 0u}); ldswf(rwa, rstd);
                ldsw64(cwa, split_c(swapadd(ca[0] + cb[0])));
                mf[2] = mf[0]; mf[3] = mf[1];
                LBAR();
                STAMP(23);
                const u32x4 c0 = ldsr128(cra);
                f32x4 rs[4];
                if (V) {
#pragma unroll
                    for (int e = 0; e < 4; ++e) rs[e] = ldsrf4(rra + 32 * e);
                }
                if (V) a0 = MFMA32(mf, c0, a0); else a0 = MFMA32(c0, mf, a0);
                tag += 1; PINV(tag);
                fin(V, a0, rstd, rs, T0, so0);
            };
            if (isv) bodyB(std::true_type{}); else bodyB(std::false_type{});
            __builtin_amdgcn_s_setprio(0);
            STAMP(2);
            WAITBAR(0);
            if (w == 7) publish(ws, WS_FA1 + (g * 16 + i) * 128, tag, lane);
            STAMP(3);
        }
    }
    const unsigned i3 = 2 * xcd + (bq >> 4), j3 = bq & 15;
    const unsigned i4 = P4MAP == 2 ? 8 * (xcd >> 2) + (bq >> 2) : P4MAP == 1 ? 4 * (xcd >> 1) + (bq >> 3) : i3, j4 = P4MAP == 2 ? 4 * (xcd & 3) + (bq & 3) : P4MAP == 1 ? 8 * (xcd & 1) + (bq & 7) : j3;
    const unsigned rot4 = ROT4;
    const unsigned ct3 = 7 * (j3 & 3) + min(w, 6u);
    const unsigned wvo = w < 7 ? l16 : OOB;
    u32x4 W[29];
    constexpr int NW4A = NW4ATT;
    const unsigned w4b = WS_W4 + (7 * j4 + min(w, 6u)) * 29 * 1024;
    auto w4load = [&](int p) AI { unsigned c = rot4 + (p >> 2); c = p == 28 ? 7 : c >= 7 ? c - 7 : c; W[p] = ldc(rWs, wvo + (p & 3) * 1024, w4b + c * 4096); };
    {
        const unsigned h = 2 * xcd + (bq >> 4), i = bq & 15;
        const unsigned u = w & 3, kh = w >> 2;
        const unsigned kvs = WS_KV + (h * 64 + 32 * kh + u) * 4096;
        auto kvsrc = [&](unsigned stg) AI { return kvs + ((stg + KVROT) & 7) * 16384; };
        unsigned k0s = kvsrc(0) - u * 4096 + u * 2048, qs = WS_QF + (h * 64 + 4 * i) * 2048 + w * 1024;
        constexpr unsigned QB = 3 * 32768;
        PINS(k0s); PINS(qs);
        __builtin_amdgcn_s_waitcnt(0x0F70);
        u32x4 rv[7]; u32x2 rcs;
        unsigned rdst = 0, rdstep = 0, rvo2 = OOB, rcv = OOB;
        if (w < 4 && rt_type < 3) {
            rvo2 = l16;
            const unsigned q4 = i16 >> 2, p4 = i16 & 3, u2 = bid - 112;
            const unsigned la = LB + RTB + (rt_type == 0 ? (g4 & 1) * 14336 + (8 * hh + q4) * 128 + p4 * 8 + w * 32
                                          : rt_type == 1 ? (4 * hh + q4) * 128 + (g4 & 1) * 32 + p4 * 8 + (w & 1) * 64 + (w >> 1) * 14336 : (16 * (g4 >> 1) + 4 * (g4 & 1) + q4) * 896 + p4 * 8 + w * 224);
            const unsigned d2 = rt_type == 0 ? 512 : rt_type == 1 ? 1024 : 7168, fo = rt_type == 2 ? 32 : 2048;
            rdst = rt_type == 0 ? WS_W4 + ((4 * (bid >> 2) + w) * 29 + 7 * (bid & 3)) * 1024 : rt_type == 1 ? WS_WD + ((2 * (u2 >> 3) + (w & 1)) * 112 + 14 * (u2 & 7) + 7 * (w >> 1)) * 1024 : WS_WO + (112 * w + ((bid - 168) & 15)) * 1024;
            rdstep = rt_type == 2 ? 16384 : 1024;
            rcv = rt_type == 0 && hh == 0 ? (28 - 7 * (bid & 3)) * 1024 + (t32 + 32 * ((bid >> 1) & 1)) * 16 + (bid & 1) * 8 : OOB;
            f32x4 ca = {}, cb = {};
#pragma unroll
            for (int e = 0; e < 7; ++e) {
                rv[e] = ldstr2(la + e * fo, la + e * fo + d2);
                if (rt_type == 2 && g4 == 3) { rv[e][2] = 0; rv[e][3] = 0; }
                msum(rv[e], ca, cb);
            }
            rcs = split_c(swapadd(ca[0] + cb[0]));
            PINS(rdst); PINS(rdstep); PINV(rcv); PINV(rcs);
        }
        if (w == 7) waitf(ws, WS_FA1 + (lane < 32 ? ((16 + 16 * g4 + h) / 3 * 16 + i16) : (h / 3 * 16 + i)) * 128, tag);
        LBAR();
        STAMP(4);
        dma1c(rWs, l16, qs, LB + QB + w * 1024);
        dma2c(rWs, l16, k0s, LB + kh * 16384 + u * 2048);
        dma2c(rWs, l16, k0s + 8192, LB + kh * 16384 + 8192 + u * 2048);
        WAITBAR(2);
        STAMP(5);
        const u32x4 q0 = ldsr128(LB + QB + u * 2048 + l16);
        u32x4 q1 = ldsr128(LB + QB + u * 2048 + 1024 + l16);
        const unsigned rb = LB + kh * 16384 + l16, rb2 = rb + 65536, rb4 = rb + 131072;
        auto ta = [&](int t) AI { const int sl = (t >> 2) % 5, off = (t & 3) * 4096; return sl < 2 ? rb + sl * 32768 + off : sl < 4 ? rb2 + (sl - 2) * 32768 + off : rb4 + off; };
        f32x16 o = {}, S[2];
        u32x4 kf0, kf1, vf0, vf1, p0, p1 = {0u, 0u, 0u, 0u}, fa0, fa1;
        float e14 = 0.f, e15 = 0.f;
        const f32x16 z16 = {};
        {
            kf0 = ldsr128(LB + l16); kf1 = ldsr128(LB + l16 + 1024);
            const u32x4 ka = ldsr128(ta(0)), kb = ldsr128(ta(0) + 1024);
            SBAR();
            dma4c(rWs, l16, kvsrc(1), LB + 32768 + w * 4096);
            f32x16 sl = MFMA32(kf0, q0, z16); sl = MFMA32(kf1, q1, sl);
            S[0] = MFMA32(ka, q0, z16);
            kf0 = ldsr128(ta(1)); kf1 = ldsr128(ta(1) + 1024); vf0 = ldsr128(ta(0) + 2048); vf1 = vf0;
            float m = fmaxf(sl[0], sl[1]);
#pragma unroll
            for (int e = 2; e < 16; e += 2) m = fmaxf(fmaxf(m, sl[e]), sl[e + 1]);
            m = swapmax(m);
            if (hh) q1[2] = pk2(-m, 0.f);
            S[0] = MFMA32(kb, q1, S[0]);
            SBAR();
#pragma unroll
            for (int e = 0; e < 7; ++e) stc(rv[e], rWs, rvo2, rdst + e * rdstep);
            __builtin_amdgcn_raw_buffer_store_b64(rcs, rWs, rcv, rdst, SC1);
        }
#define EX(v) __builtin_amdgcn_exp2f(v)
        auto wq = [](int t) constexpr { return t >= 18 ? 4 + 2 * (t - 18) : t >= 14 ? t - 15 : 99; };
        static_for<32>([&](auto TT) AI {
            constexpr int t = TT;
            f32x16& SC = S[t & 1]; f32x16& SN = S[(t + 1) & 1];
            if (t == 0 || t == 2) VBAR(12);
            if (t == 6) { VBAR(0); if (w == 7) publish(ws, WS_FA2 + bid * 8, tag, lane); }
            if (t == 14) {
                asm volatile("s_waitcnt vmcnt(0)" : "+v"(fa0), "+v"(fa1) :: "memory");
                if (w == 7) {
                    const unsigned tl = (unsigned)tag, th = (unsigned)(tag >> 32);
#pragma nounroll
                    for (unsigned spin = 0; spin < SPIN_CAP; ++spin) {
                        const bool ok = fa0[0] == tl && fa0[1] == th && fa0[2] == tl && fa0[3] == th && fa1[0] == tl && fa1[1] == th && fa1[2] == tl && fa1[3] == th;
                        if (__all(ok)) break;
                        __builtin_amdgcn_s_sleep(2);
                        FENCE();
                        fa0 = ldc(rWs, l16, WS_FA2); fa1 = ldc(rWs, l16, WS_FA2 + 1024);
                        __builtin_amdgcn_s_waitcnt(0x0F70);
                    }
                }
                BAR();
            }
            if (t == 22) VBAR(9);
            if (t == 0 || t == 2 || t == 6 || t == 14 || t == 22) STAMP(32 + t);
            __builtin_amdgcn_s_setprio(3 - (t & 3));
            float a0_, a1_, a2_, a3_;
            u32x4 nk0, nk1, nv0, nv1;
            __builtin_amdgcn_s_waitcnt(0xC07F);
            SBAR();
            if (t < 31) SN = MFMA32(kf0, q0, z16);
            SBAR();
            nv1 = ldsr128(ta(t) + 3072);
            if (t < 30) nk0 = ldsr128(ta(t + 2));
            SBAR();
            a0_ = EX(SC[0]); a1_ = EX(SC[1]); SBAR(); p1[3] = pk2(e14, e15); SBAR(); a2_ = EX(SC[2]); a3_ = EX(SC[3]); SBAR(); p0[0] = pk2(a0_, a1_); SBAR();
            o = MFMA32(vf1, p1, o); SBAR();
            if (t < 30) nk1 = ldsr128(ta(t + 2) + 1024);
            if (t < 31) nv0 = ldsr128(ta(t + 1) + 2048);
            SBAR();
            a0_ = EX(SC[4]); a1_ = EX(SC[5]); SBAR(); p0[1] = pk2(a2_, a3_); SBAR(); a2_ = EX(SC[6]); a3_ = EX(SC[7]); SBAR(); p0[2] = pk2(a0_, a1_); SBAR();
            if (t < 31) SN = MFMA32(kf1, q1, SN);
            SBAR();
            if (t < 4) dma2c(rWs, l16, kvsrc(2 + (t >> 1)) + (t & 1) * 2048, LB + (2 + (t >> 1)) * 32768 + w * 4096 + (t & 1) * 2048);
            else if (t >= 6 && t < 10) dma2c(rWs, l16, kvsrc(1 + (t >> 1)) + (t & 1) * 2048, LB + (t < 8 ? 4 * 32768 : 0) + w * 4096 + (t & 1) * 2048);
            else if (t >= 14 && t < 18) dma2c(rWs, l16, kvsrc(6 + ((t - 14) >> 1)) + (t & 1) * 2048, LB + (1 + ((t - 14) >> 1)) * 32768 + w * 4096 + (t & 1) * 2048);
            else if (wq(t) < NW4A) w4load(wq(t));
            SBAR();
            a0_ = EX(SC[8]); a1_ = EX(SC[9]); SBAR(); p0[3] = pk2(a2_, a3_); SBAR(); a2_ = EX(SC[10]); a3_ = EX(SC[11]); SBAR(); p1[0] = pk2(a0_, a1_); SBAR();
            o = MFMA32(vf0, p0, o); SBAR();
            if (wq(t) + 1 < NW4A) w4load(wq(t) + 1);
            if (t == 12) asm volatile("buffer_load_dwordx4 %0, %2, %3, %4 offen sc1\n\tbuffer_load_dwordx4 %1, %2, %3, %4 offen offset:1024 sc1"
                                      : "=&v"(fa0), "=&v"(fa1) : "v"(w == 7 ? l16 : OOB), "s"(rWs), "s"(WS_FA2) : "memory");
            SBAR();
            a0_ = EX(SC[12]); a1_ = EX(SC[13]); SBAR(); p1[1] = pk2(a2_, a3_); SBAR(); e14 = EX(SC[14]); e15 = EX(SC[15]); SBAR(); p1[2] = pk2(a0_, a1_); SBAR();
            if (t == 5 || t == 13) { PINV(e14); PINV(e15); PINV(p1); }
            vf1 = nv1;
            if (t < 30) { kf0 = nk0; kf1 = nk1; }
            if (t < 31) vf0 = nv0;
        });
        __builtin_amdgcn_s_setprio(0);
        p1[3] = pk2(e14, e15); o = MFMA32(vf1, p1, o);
        STAMP(6);
        constexpr unsigned EB = 3 * 32768;
        {
            const unsigned mine = LB + EB + w * 4096, theirs = LB + EB + (w ^ 4) * 4096;
            f32x4 ka, kb, sa4, sb4;
            if (kh) { sa4 = (f32x4){o[0], o[1], o[2], o[3]}; sb4 = (f32x4){o[4], o[5], o[6], o[7]}; ka = (f32x4){o[8], o[9], o[10], o[11]}; kb = (f32x4){o[12], o[13], o[14], o[15]}; }
            else { ka = (f32x4){o[0], o[1], o[2], o[3]}; kb = (f32x4){o[4], o[5], o[6], o[7]}; sa4 = (f32x4){o[8], o[9], o[10], o[11]}; sb4 = (f32x4){o[12], o[13], o[14], o[15]}; }
            ldswf4(mine + l16, sa4); ldswf4(mine + 1024 + l16, sb4); ldswf(mine + 2048 + lane * 4, o[12]);
            LBAR();
            STAMP(48);
            ka += ldsrf4(theirs + l16); kb += ldsrf4(theirs + 1024 + l16);
            float l = hi2all(o[12] + ldsrf(theirs + 2048 + lane * 4));
            float am = fmaxf(fmaxf(fmaxf(fabsf(ka[0]), fabsf(ka[1])), fmaxf(fabsf(ka[2]), fabsf(ka[3]))), fmaxf(fmaxf(fabsf(kb[0]), fabsf(kb[1])), fmaxf(fabsf(kb[2]), fabsf(kb[3]))));
            const bool good = l >= 0x1p-40f && l <= 0x1p60f && am <= 0x1p120f;
            if (__builtin_expect(!__all(good), 0)) {
                const unsigned kvb = WS_KV + h * 64 * 4096;
                float mx = -INFINITY;
#pragma nounroll
                for (unsigned t = 0; t < 64; ++t) {
                    f32x16 s = MFMA32(ldc(rWs, l16, kvb + t * 4096), q0, z16); s = MFMA32(ldc(rWs, l16, kvb + t * 4096 + 1024), q1, s);
#pragma unroll
                    for (int e = 0; e < 16; ++e) mx = fmaxf(mx, s[e]);
                }
                mx = swapmax(mx);
                o = z16;
#pragma nounroll
                for (unsigned t = 0; t < 64; ++t) {
                    f32x16 s = MFMA32(ldc(rWs, l16, kvb + t * 4096), q0, z16); s = MFMA32(ldc(rWs, l16, kvb + t * 4096 + 1024), q1, s);
#pragma unroll
                    for (int e = 0; e < 16; ++e) s[e] = __builtin_amdgcn_exp2f(s[e] - mx);
                    o = MFMA32(ldc(rWs, l16, kvb + t * 4096 + 2048), pack8(s, 0), o); o = MFMA32(ldc(rWs, l16, kvb + t * 4096 + 3072), pack8(s, 1), o);
                }
                l = hi2all(o[12]);
                if (kh) { ka = (f32x4){o[8], o[9], o[10], o[11]}; kb = (f32x4){o[12], o[13], o[14], o[15]}; }
                else { ka = (f32x4){o[0], o[1], o[2], o[3]}; kb = (f32x4){o[4], o[5], o[6], o[7]}; }
            }
            const float rl = __builtin_amdgcn_rcpf(l);
            u32x4 of = {pk2(ka[0] * rl, ka[1] * rl), pk2(ka[2] * rl, ka[3] * rl), pk2(kb[0] * rl, kb[1] * rl), pk2(kb[2] * rl, kb[3] * rl)};
            if (kh && hh) { of[2] = 0; of[3] = 0; }
            stc(of, rWs, (t32 >> 4) * 16384 + (i16 + 16 * hh) * 16, WS_OF + ((8 * i + 2 * u) * 16 + h) * 1024 + kh * 512);
        }
        STAMP(49);
        DRAINBAR();
        if (w == 7) publish(ws, WS_FB + (i * 16 + h) * 128, tag, lane);
        STAMP(7);
    }
    {
        const unsigned rt32 = 4 * i3 + (j3 >> 2);
        if (w < 7) {
            u32x4 wf[16];
#pragma unroll
            for (int e = 0; e < 16; ++e) wf[e] = ldc(rWs, l16 + (e & 3) * 1024, WS_WO + (ct3 * 16 + (e & ~3)) * 1024);
            const u32x2 xr0 = *(const GAS u32x2*)(x + (32 * rt32 + i16) * 896 + ct3 * 32 + g4 * 8);
            const u32x2 xr1 = *(const GAS u32x2*)(x + (32 * rt32 + 16 + i16) * 896 + ct3 * 32 + g4 * 8);
            unsigned vo = (i16 + 32 * (g4 >> 1)) * 16 + (g4 & 1) * 8, so = WS_HF + (rt32 * 28 + ct3) * 1024, sto = WS_ST + (i3 * 28 + ct3) * 1024 + (j3 >> 2) * 256;
            PINV(vo); PINS(so); PINS(sto);
            DRAINBAR();
            STAMP(8);
            f32x4 d0 = {}, d1 = {};
            u32x4 of[10];
#pragma unroll
            for (int e = 0; e < 5; ++e) { of[2 * e] = ldsr128(LB + e * 1024 + l16); of[2 * e + 1] = ldsr128(LB + 16384 + e * 1024 + l16); }
            SBAR();
#pragma unroll
            for (int e = 0; e < 16; ++e) {
                if (e == 3) BAR();
                d0 = MFMA16(wf[e], of[2 * (e % 5)], d0); d1 = MFMA16(wf[e], of[2 * (e % 5) + 1], d1);
                SBAR();
                if (e < 11) { of[2 * (e % 5)] = ldsr128(LB + (e + 5) * 1024 + l16); of[2 * (e % 5) + 1] = ldsr128(LB + 16384 + (e + 5) * 1024 + l16); }
                SBAR();
            }
            STAMP(45);
            const u32x2 h0 = {pk2(d0[0] + bflo(xr0[0]), d0[1] + bfhi(xr0[0])), pk2(d0[2] + bflo(xr0[1]), d0[3] + bfhi(xr0[1]))};
            const u32x2 h1 = {pk2(d1[0] + bflo(xr1[0]), d1[1] + bfhi(xr1[0])), pk2(d1[2] + bflo(xr1[1]), d1[3] + bfhi(xr1[1]))};
            __builtin_amdgcn_raw_buffer_store_b64(h0, rWs, vo, so, SC1);
            __builtin_amdgcn_raw_buffer_store_b64(h1, rWs, vo + 256, so, SC1);
            const float a0 = bflo(h0[0]), a1 = bfhi(h0[0]), a2 = bflo(h0[1]), a3 = bfhi(h0[1]), b0 = bflo(h1[0]), b1 = bfhi(h1[0]), b2 = bflo(h1[1]), b3 = bfhi(h1[1]);
            const float sA = (a0 + a1) + (a2 + a3), sB = (a0 * a0 + a1 * a1) + (a2 * a2 + a3 * a3), sC = (b0 + b1) + (b2 + b3), sD = (b0 * b0 + b1 * b1) + (b2 * b2 + b3 * b3);
            const auto rx = __builtin_amdgcn_permlane32_swap(__float_as_uint(sA), __float_as_uint(sC), false, false);
            const auto ry = __builtin_amdgcn_permlane32_swap(__float_as_uint(sB), __float_as_uint(sD), false, false);
            const auto rz = __builtin_amdgcn_permlane16_swap(__float_as_uint(__uint_as_float(rx[0]) + __uint_as_float(rx[1])), __float_as_uint(__uint_as_float(ry[0]) + __uint_as_float(ry[1])), false, false);
            __builtin_amdgcn_raw_buffer_store_b32(__float_as_uint(__uint_as_float(rz[0]) + __uint_as_float(rz[1])), rWs, lane * 4, sto, SC1);
            DRAINBAR();
            static_for<29 - NW4A>([&](auto PP) AI { w4load(NW4A + PP); });
            FENCE();
        } else {
            waitf(ws, WS_FB + (i3 * 16 + i16) * 128, tag);
            STAMP(47);
#pragma unroll
            for (int e = 0; e < 8; ++e) { const int pc = (e & 1) * 4 + (e >> 1); dma4c(rWs, l16, WS_OF + rt32 * 32768 + pc * 4096, LB + pc * 4096); }
            VBAR(16);
            STAMP(8);
            VBAR(0);
            DRAINBAR();
            publish(ws, WS_FC + (i3 * 16 + j3) * 128, tag, lane);
            if (bid == 0) publish(ws, WS_E, tag, lane);
        }
        STAMP(9);
    }
    u32x4 Wd[28];
    const unsigned m5 = j3 / 7, n5 = j3 % 7;
    const unsigned rot5 = ROT5;
    {
        constexpr unsigned SB = 114688;
        const unsigned wdb = WS_WD + ((2 * n5 + (w & 1)) * 112 + 28 * (w >> 1)) * 1024, wdv = j3 < 14 ? l16 : OOB;
        auto wdload = [&](int p) AI { unsigned s = rot5 + (p >> 2); s = s >= 7 ? s - 7 : s; Wd[p] = ldc(rWs, wdv + (p & 3) * 1024, wdb + s * 4096); };
        if (w == 7) {
            waitf(ws, WS_FC + (i4 * 16 + i16) * 128, tag);
            STAMP(10);
            unsigned c = rot4;
            auto chunk = [&](int pc) AI {
#pragma unroll
                for (int a = 0; a < 4; ++a) dma4c(rWs, l16, WS_HF + ((4 * i4 + a) * 28 + 4 * c) * 1024, LB + pc * 16384 + a * 4096);
                c = c == 6 ? 0 : c + 1;
            };
            chunk(0); chunk(1);
            WAITBAR(16);
            chunk(2); chunk(3);
            WAITBAR(16);
            chunk(4); chunk(5); chunk(6);
            f32x4 sv[28];
#pragma unroll
            for (int e = 0; e < 28; ++e) sv[e] = __builtin_bit_cast(f32x4, ldc(rWs, l16 + (e & 3) * 1024, WS_ST + (i4 * 28 + (e & ~3)) * 1024));
            WAITBAR(28);
            f32x4 sum = sv[0];
#pragma unroll
            for (int e = 1; e < 28; ++e) sum += sv[e];
            ldswf4(LB + SB + l16, sum);
#pragma unroll
            for (int p = 0; p < 28; ++p) wdload(p);
            LBAR();
        } else {
            STAMP(10);
            f32x16 acc[4] = {};
            const unsigned ha = LB + l16, hb = ha + 65536;
            auto hadr = [&](int p, int a) AI { const int off = (p >> 2) * 16384 + a * 4096 + (p & 3) * 1024; return off < 65536 ? ha + off : hb + (off - 65536); };
            u32x4 hf[2][4];
            float r2[4]; u32x4 mf[4];
            LBAR();
#pragma unroll
            for (int a = 0; a < 4; ++a) hf[0][a] = ldsr128(hadr(0, a));
            static_for<28>([&](auto PP) AI {
                constexpr int p = PP;
                if (p == 3 || p == 11 || p == 22) asm volatile("s_barrier" ::: "memory");
                if (p == 0) STAMP(33); if (p == 3) STAMP(35); if (p == 11) STAMP(36); if (p == 22) STAMP(37);
#pragma unroll
                for (int a = 0; a < 4; ++a) {
                    acc[a] = MFMA32(W[p], hf[p & 1][a], acc[a]);
                    if (p < 27) hf[(p + 1) & 1][a] = ldsr128(hadr(p + 1, a));
                    if constexpr (p >= 23 && p < 27) if (a == 0) {
                        const unsigned sa = LB + SB + ((p - 23) * 64 + (t32 >> 4) * 32 + i16) * 4;
                        const float mean = ldsrf(sa) * (1.f / 448.f), var = fmaxf(ldsrf(sa + 64) * (1.f / 448.f) - mean * mean, 0.f);
                        r2[p - 23] = __builtin_amdgcn_rcpf(var + LN_EPS); const u32x4 m4 = corr_m(mean); mf[p - 23] = (u32x4){m4[0], m4[1], m4[0], m4[1]};
                    }
                    SBAR();
                }
                if (p >= 8 && p < 16) { wdload(2 * (p - 8)); wdload(2 * (p - 8) + 1); }
                if (p >= 16) wdload(p);
                SBAR();
            });
#pragma unroll
            for (int a = 0; a < 4; ++a) acc[a] = MFMA32(W[28], mf[a], acc[a]);
            STAMP(39);
#pragma unroll
            for (int a = 0; a < 4; ++a) {
                float gv[8];
#pragma unroll
                for (int e = 0; e < 8; ++e) gv[e] = fmaxf(acc[a][e] * r2[a], 0.f) * acc[a][e + 8];
                stc((u32x4){pk2(gv[0], gv[1]), pk2(gv[2], gv[3]), pk2(gv[4], gv[5]), pk2(gv[6], gv[7])}, rWs, l16, WS_GF + ((4 * i4 + a) * 112 + 7 * j4 + w) * 1024);
            }
        }
        STAMP(11);
        DRAINBAR();
        if (w == 7) { publish(ws, WS_FD + (i4 * 16 + j4) * 128, tag, lane); if (j3 < 14) waitf(ws, WS_FD + (i3 * 16 + i16) * 128, tag); }
        STAMP(12);
    }
    if (j3 < 14) {
        unsigned hvo = (tid >> 8) * 28672 + ((tid & 7) >> 1) * 1024 + (((tid >> 3) & 31) + 32 * (tid & 1)) * 16;
        PINV(hvo);
        LBAR();
        STAMP(13);
        const unsigned kq = w >> 1;
        const unsigned gs = WS_GF + ((4 * i3 + 2 * m5 + (w & 1)) * 112 + 28 * kq) * 1024;
        auto gsrc = [&](int s) AI { unsigned c = rot5 + s; c = c >= 7 ? c - 7 : c; return gs + c * 4096; };
        dma4c(rWs, l16, gsrc(0), LB + w * 4096);
        u32x4 hres;
        dma4c(rWs, l16, gsrc(1), LB + 32768 + w * 4096);
        f32x16 acc[2] = {};
        const unsigned ga = LB + kq * 8192 + l16, gb = ga + 65536;
#pragma unroll
        for (int s = 0; s < 7; ++s) {
            if (s < 6) WAITBAR(4); else WAITBAR(1);
            if (s == 5) asm volatile("buffer_load_dwordx4 %0, %1, %2, %3 offen sc1" : "=&v"(hres) : "v"(hvo), "s"(rWs), "s"(WS_HF + ((4 * i3 + 2 * m5) * 28 + 4 * n5) * 1024) : "memory");
            if (s == 0) STAMP(40); if (s == 1) STAMP(41); if (s == 3) STAMP(42); if (s == 6) STAMP(44);
            const unsigned a = s % 3 == 2 ? gb : ga + (s % 3) * 32768;
            u32x4 gf[8];
#pragma unroll
            for (int e = 0; e < 4; ++e) { gf[2 * e] = ldsr128(a + e * 1024); gf[2 * e + 1] = ldsr128(a + 4096 + e * 1024); }
            SBAR();
#pragma unroll
            for (int e = 0; e < 4; ++e) {
                if (s < 5) dma1c(rWs, l16, gsrc(s + 2) + e * 1024, LB + ((s + 2) % 3) * 32768 + w * 4096 + e * 1024);
                acc[0] = MFMA32(Wd[4 * s + e], gf[2 * e], acc[0]);
                acc[1] = MFMA32(Wd[4 * s + e], gf[2 * e + 1], acc[1]);
                SBAR();
            }
        }
        STAMP(14);
        constexpr unsigned RB = 3 * 32768;
#pragma unroll
        for (int m = 0; m < 2; ++m)
#pragma unroll
            for (int e = 0; e < 4; ++e)
                ldswf4(LB + RB + kq * 16384 + (32 * m + t32) * 256 + (((8 * (w & 1) + 2 * e + hh) ^ (t32 & 15)) << 4), (f32x4){acc[m][4 * e], acc[m][4 * e + 1], acc[m][4 * e + 2], acc[m][4 * e + 3]});
        asm volatile("s_waitcnt vmcnt(0) lgkmcnt(0)\n\ts_barrier" : "+v"(hres) :: "memory");
        const unsigned row = tid >> 3, c8 = tid & 7;
        f32x4 lo = {bflo(hres[0]), bfhi(hres[0]), bflo(hres[1]), bfhi(hres[1])}, hi = {bflo(hres[2]), bfhi(hres[2]), bflo(hres[3]), bfhi(hres[3])};
        f32x4 slo = {}, shi = {};
#pragma unroll
        for (int k = 0; k < 4; ++k) {
            slo += ldsrf4(LB + RB + k * 16384 + row * 256 + (((2 * c8) ^ (row & 15)) << 4));
            shi += ldsrf4(LB + RB + k * 16384 + row * 256 + (((2 * c8 + 1) ^ (row & 15)) << 4));
        }
        lo += slo; hi += shi;
        *(GAS u32x4*)(out + (128 * i3 + 64 * m5 + row) * 896 + n5 * 128 + c8 * 16) = (u32x4){pk2(lo[0], lo[1]), pk2(lo[2], lo[3]), pk2(hi[0], hi[1]), pk2(hi[2], hi[3])};
    }
}

extern "C" void kernel_launch(void* const* d_in, const int* in_sizes, int n_in, void* d_out, int out_size, void* d_ws, size_t ws_size, hipStream_t stream) {
    block_fwd<<<256, 512, 0, stream>>>((const char*)d_in[0], (const char*)d_in[1], (const char*)d_in[2], (const char*)d_in[4], (const char*)d_in[5], (const char*)d_in[6],
                                       (char*)d_out, (char*)d_ws);
}
```

```cpp
#include <hip/hip_runtime.h>
#include <stdint.h>
#include <type_traits>
#include <utility>

typedef __attribute__((ext_vector_type(8))) __bf16 bf16x8;
typedef __attribute__((ext_vector_type(2))) __bf16 bf16x2;
typedef __attribute__((ext_vector_type(2))) float f32x2;
typedef __attribute__((ext_vector_type(4))) float f32x4;
typedef __attribute__((ext_vector_type(16))) float f32x16;
typedef __attribute__((ext_vector_type(4))) unsigned u32x4;
typedef __attribute__((ext_vector_type(2))) unsigned u32x2;
typedef __attribute__((ext_vector_type(4))) short s16x4;
typedef unsigned long long u64;
typedef __amdgpu_buffer_rsrc_t rsrc_t;
#define DI __device__ __forceinline__
#define LAS __attribute__((address_space(3)))
#define GAS __attribute__((address_space(1)))
#define RLX_AGENT __ATOMIC_RELAXED, __HIP_MEMORY_SCOPE_AGENT
#define SC1 16

constexpr unsigned WS_E = 0;
constexpr unsigned WS_FA = 4096;
constexpr unsigned WS_FB = WS_FA + 32768;
constexpr unsigned WS_FC = WS_FB + 32768;
constexpr unsigned WS_FD = WS_FC + 32768;
constexpr unsigned WS_FW = WS_FD + 65536;
constexpr unsigned WS_QF = 262144;
constexpr unsigned WS_KV = WS_QF + (2u << 20);
constexpr unsigned WS_OF = WS_KV + (4u << 20);
constexpr unsigned WS_HF = WS_OF + (2u << 20);
constexpr unsigned WS_GF = WS_HF + 64u * 28 * 1024;
constexpr unsigned WS_W4 = WS_GF + 64u * 112 * 1024;
constexpr unsigned WS_WO = WS_W4 + 112u * 29 * 1024;
constexpr unsigned WS_WD = WS_WO + 28u * 16 * 1024;
constexpr unsigned WS_ST = WS_WD + 14u * 112 * 1024;
constexpr unsigned WS_S1 = WS_ST + 16u * 28 * 1024;
constexpr unsigned WS_END = WS_S1 + 16u * 14 * 2048;
constexpr unsigned OOB = 0x80000000u;

#define P4B 20
#define WOSLP 4
constexpr float QSCALE = 0.18898223650461362f * 1.4426950408889634f;
constexpr unsigned CODE_AHEAD = 35840;
constexpr float LN_EPS = 1e-5f;

__shared__ __attribute__((aligned(16))) char lds[163840];

#define MFMA32(a, b, c) __builtin_amdgcn_mfma_f32_32x32x16_bf16(__builtin_bit_cast(bf16x8, a), __builtin_bit_cast(bf16x8, b), c, 0, 0, 0)
#define MFMA16(a, b, c) __builtin_amdgcn_mfma_f32_16x16x32_bf16(__builtin_bit_cast(bf16x8, a), __builtin_bit_cast(bf16x8, b), c, 0, 0, 0)
#define SBAR() __builtin_amdgcn_sched_barrier(0)
#define WAITBAR(N) asm volatile("s_waitcnt vmcnt(" #N ") lgkmcnt(0)\n\ts_barrier" ::: "memory")
#define DRAINBAR() do { __builtin_amdgcn_s_waitcnt(0x0070); WAITBAR(0); } while (0)
#define LBAR() asm volatile("s_waitcnt lgkmcnt(0)\n\ts_barrier" ::: "memory")
#define BAR() asm volatile("s_barrier" ::: "memory")
#define VBAR(N) asm volatile("s_waitcnt vmcnt(" #N ")\n\ts_barrier" ::: "memory")
#define VBARN(N) asm volatile("s_waitcnt vmcnt(%0)\n\ts_barrier" ::"n"(N) : "memory")
#define FENCE() asm volatile("" ::: "memory")

template <int... I, class F> DI void static_for_impl(std::integer_sequence<int, I...>, F&& f) { (f(std::integral_constant<int, I>{}), ...); }
template <int N, class F> DI void static_for(F&& f) { static_for_impl(std::make_integer_sequence<int, N>{}, f); }
#define AI __attribute__((always_inline))
DI unsigned rfl(unsigned v) { return __builtin_amdgcn_readfirstlane(v); }
DI unsigned pk2(float lo, float hi) { f32x2 v = {lo, hi}; bf16x2 b = __builtin_convertvector(v, bf16x2); return __builtin_bit_cast(unsigned, b); }
DI float bflo(unsigned v) { return __uint_as_float(v << 16); }
DI float bfhi(unsigned v) { return __uint_as_float(v & 0xffff0000u); }
DI u32x4 ldsr128(unsigned a) { return *(const LAS u32x4*)(size_t)a; }
DI f32x4 ldsrf4(unsigned a) { return *(const LAS f32x4*)(size_t)a; }
DI float ldsrf(unsigned a) { return *(const LAS float*)(size_t)a; }
DI void ldsw128(unsigned a, u32x4 v) { *(LAS u32x4*)(size_t)a = v; }
DI void ldswf4(unsigned a, f32x4 v) { *(LAS f32x4*)(size_t)a = v; }
DI void ldswf(unsigned a, float v) { *(LAS float*)(size_t)a = v; }
DI void ldsw64(unsigned a, u32x2 v) { *(LAS u32x2*)(size_t)a = v; }
DI u32x2 ldstr(unsigned a) { return __builtin_bit_cast(u32x2, __builtin_amdgcn_ds_read_tr16_b64_v4i16((LAS s16x4*)(size_t)a)); }
DI u32x4 ldstr2(unsigned a, unsigned b) { u32x2 lo = ldstr(a), hi = ldstr(b); return (u32x4){lo[0], lo[1], hi[0], hi[1]}; }
DI float swapadd(float v) { auto r = __builtin_amdgcn_permlane32_swap(__float_as_uint(v), __float_as_uint(v), false, false); return __uint_as_float(r[0]) + __uint_as_float(r[1]); }
DI float swapmax(float v) { auto r = __builtin_amdgcn_permlane32_swap(__float_as_uint(v), __float_as_uint(v), false, false); return fmaxf(__uint_as_float(r[0]), __uint_as_float(r[1])); }
DI float hi2all(float v) { auto r = __builtin_amdgcn_permlane32_swap(__float_as_uint(v), __float_as_uint(v), false, false); return __uint_as_float(r[1]); }
#define MFMA4(a, b, c) __builtin_amdgcn_mfma_f32_4x4x4bf16_1k(__builtin_bit_cast(s16x4, a), __builtin_bit_cast(s16x4, b), c, 0, 0, 0)
DI void msum(u32x4 f, f32x4& c, f32x4& d) { const u32x2 one = {0x3f803f80u, 0x3f803f80u}, lo = {f[0], f[1]}, hi = {f[2], f[3]}; c = MFMA4(one, lo, c); d = MFMA4(one, hi, d); }
DI void msq(u32x4 f, f32x4& c, f32x4& d) { const u32x2 lo = {f[0], f[1]}, hi = {f[2], f[3]}; c = MFMA4(lo, lo, c); d = MFMA4(hi, hi, d); }
#define PINV(x) asm volatile("" : "+v"(x))
#define PINS(x) do { x = rfl(x); asm volatile("" : "+s"(x)); } while (0)
DI u32x4 pack8(const f32x16& a, int s) { return (u32x4){pk2(a[8 * s], a[8 * s + 1]), pk2(a[8 * s + 2], a[8 * s + 3]), pk2(a[8 * s + 4], a[8 * s + 5]), pk2(a[8 * s + 6], a[8 * s + 7])}; }
DI u32x2 split_c(float c) { const unsigned h = pk2(c, c); return (u32x2){h, pk2(c - bfhi(h), 0.f)}; }
DI u32x2 split_m(float m) { const float n = -m; const unsigned h = pk2(n, n); return (u32x2){pk2(bfhi(h), n - bfhi(h)), h & 0xffffu}; }

DI void dma4v(rsrc_t r, unsigned v0, unsigned v1, unsigned v2, unsigned v3, unsigned soff, unsigned m0v) {
    asm volatile("s_mov_b32 m0, %0\n\ts_nop 0\n\tbuffer_load_dwordx4 %1, %5, %6 offen lds\n\tbuffer_load_dwordx4 %2, %5, %6 offen offset:1024 lds\n\t"
                 "buffer_load_dwordx4 %3, %5, %6 offen offset:2048 lds\n\tbuffer_load_dwordx4 %4, %5, %6 offen offset:3072 lds" ::"s"(m0v), "v"(v0), "v"(v1), "v"(v2), "v"(v3), "s"(r), "s"(soff) : "memory");
}
DI void dma3v(rsrc_t r, unsigned v0, unsigned v1, unsigned v2, unsigned soff, unsigned m0v) {
    asm volatile("s_mov_b32 m0, %0\n\ts_nop 0\n\tbuffer_load_dwordx4 %1, %4, %5 offen lds\n\tbuffer_load_dwordx4 %2, %4, %5 offen offset:1024 lds\n\t"
                 "buffer_load_dwordx4 %3, %4, %5 offen offset:2048 lds" ::"s"(m0v), "v"(v0), "v"(v1), "v"(v2), "s"(r), "s"(soff) : "memory");
}
DI void dma2c(rsrc_t r, unsigned voff, unsigned soff, unsigned m0v) {
    asm volatile("s_mov_b32 m0, %0\n\ts_nop 0\n\tbuffer_load_dwordx4 %1, %2, %3 offen sc1 lds\n\tbuffer_load_dwordx4 %1, %2, %3 offen offset:1024 sc1 lds" ::"s"(m0v), "v"(voff), "s"(r), "s"(soff) : "memory");
}
DI void dma4c(rsrc_t r, unsigned voff, unsigned soff, unsigned m0v) {
    asm volatile("s_mov_b32 m0, %0\n\ts_nop 0\n\tbuffer_load_dwordx4 %1, %2, %3 offen sc1 lds\n\tbuffer_load_dwordx4 %1, %2, %3 offen offset:1024 sc1 lds\n\t"
                 "buffer_load_dwordx4 %1, %2, %3 offen offset:2048 sc1 lds\n\tbuffer_load_dwordx4 %1, %2, %3 offen offset:3072 sc1 lds" ::"s"(m0v), "v"(voff), "s"(r), "s"(soff) : "memory");
}
DI u32x4 ldc(rsrc_t r, unsigned voff, unsigned soff) { return __builtin_amdgcn_raw_buffer_load_b128(r, voff, soff, SC1); }
DI void stc(u32x4 v, rsrc_t r, unsigned voff, unsigned soff) { __builtin_amdgcn_raw_buffer_store_b128(v, r, voff, soff, SC1); asm volatile("s_nop 1" ::"v"(v) : "memory"); }

constexpr unsigned SPIN_CAP = 1u << 20;
DI void publish(char* ws, unsigned off, u64 tag, int lane) { if (lane == 0) __hip_atomic_store((GAS u64*)(ws + off), tag, RLX_AGENT); }
DI void waitf(char* ws, unsigned lane_off, u64 tag) {
    GAS u64* p = (GAS u64*)(ws + lane_off);
#pragma nounroll
    for (unsigned spin = 0; spin < SPIN_CAP; ++spin) {
        const u64 v = __hip_atomic_load(p, RLX_AGENT);
        if (__all(v == tag)) break;
        __builtin_amdgcn_s_sleep(1);
    }
    FENCE();
}

#define STAMP(n)
#define STAMPD(n)

__global__ __launch_bounds__(512, 2) void block_fwd(const char* __restrict__ x, const char* __restrict__ wqkv, const char* __restrict__ wo, const char* __restrict__ wg,
                                                    const char* __restrict__ wu, const char* __restrict__ wd, char* __restrict__ out, char* ws) {
    STAMP(0);
    const int tid = threadIdx.x, lane = tid & 63;
    unsigned w = rfl(tid >> 6);
    const unsigned bid = blockIdx.x, xcd = bid & 7, bq = bid >> 3;
    const unsigned t32 = lane & 31, hh = lane >> 5, g4 = lane >> 4, i16 = lane & 15;
    const unsigned LB = (unsigned)(size_t)(LAS char*)lds;
    const rsrc_t rWs = __builtin_amdgcn_make_buffer_rsrc(ws, 0, WS_END, 0x00020000);
    const unsigned l16 = lane * 16;
    u64 tag = __hip_atomic_load((GAS u64*)(ws + WS_E), RLX_AGENT);
    unsigned cpf = 0;
    if (w < 4) {
        u64 pc; asm volatile("s_getpc_b64 %0" : "=s"(pc));
        cpf = __builtin_amdgcn_raw_buffer_load_b32(__builtin_amdgcn_make_buffer_rsrc((void*)(pc & ~127ull), 0, CODE_AHEAD, 0x00020000), lane < 3 ? ((bq * 4 + w) * 3 + lane) * 128 : OOB, 0, 0);
    }
    FENCE();

    if (bq < 28) {
        const unsigned i = 4 * (xcd >> 1) + bq / 7, g = 7 * (xcd & 1) + bq % 7;
        const unsigned r = w & 3, q4 = i16 >> 2, p4 = i16 & 3, rt = 4 * i + r;
        const unsigned kc0 = (g + 2 * i) % 7;
        constexpr unsigned SLOT = 28672;
        constexpr unsigned MFB = 114688, RSB = MFB + 2048, CSB = RSB + 512;
        const unsigned p0 = w < 4 ? (g == 9 ? 1 : 0) : (g == 9 ? 0 : 2);
        const bool isv = 3 * g + p0 >= 28;
        f32x16 a0 = {}, a1 = {};
        unsigned xa[4], wa[4];
        auto rdaddr = [&]() AI {
#pragma unroll
            for (int s = 0; s < 4; ++s) {
                const unsigned sp = (s + r) & 3;
                xa[s] = LB + (32 * r + t32) * 128 + (((2 * sp + hh) ^ ((t32 >> 1) & 7)) << 4);
                wa[s] = LB + 16384 + (16 * sp + 8 * hh + q4) * 192 + p0 * 64 + (4 * (g4 & 1) + p4) * 8;
            }
        };
        struct Dst { unsigned va, vb, vx, so; bool sa, sb; };
        auto mkdst = [&](unsigned T) AI {
            Dst d; const unsigned part = T / 14, tp = T % 14;
            if (T >= 28) { const unsigned c = 32 * tp + t32, hd = c / 28; d.va = hd * 262144 + (c - 28 * hd + 32 * hh) * 16; d.vb = d.va + 1024; d.vx = OOB; d.sa = d.sb = false; d.so = WS_KV + rt * 4096 + 2048; }
            else {
                const unsigned hs = part ? 262144 : 131072;
                const unsigned ca = 4 * tp + hh, pa = ca / 7, ra = ca - 7 * pa, cb = ca + 2, pb = cb / 7, rb = cb - 7 * pb;
                const unsigned ga = ra > 3 ? ra - 3 : ra, gb = rb > 3 ? rb - 3 : rb;
                d.va = (2 * pa + (ra > 3)) * hs + (ga >> 1) * 1024 + (t32 + 32 * (ga & 1)) * 16;
                d.vb = (2 * pb + (rb > 3)) * hs + (gb >> 1) * 1024 + (t32 + 32 * (gb & 1)) * 16;
                d.sa = ra == 3; d.sb = rb == 3;
                d.vx = d.sa ? (2 * pa + 1) * hs + t32 * 16 : d.sb ? (2 * pb + 1) * hs + t32 * 16 : OOB;
                d.so = part ? WS_KV + rt * 4096 : WS_QF + rt * 2048;
            }
            PINV(d.va); PINV(d.vb); PINV(d.vx); PINS(d.so);
            return d;
        };
        auto fin = [&](auto V, f32x16 a, float rstd, const f32x4* rs, unsigned T, const Dst& d) AI {
            if (V) {
#pragma unroll
                for (int e = 0; e < 16; ++e) a[e] *= rs[e >> 2][e & 3];
                stc(pack8(a, 0), rWs, d.va, d.so); stc(pack8(a, 1), rWs, d.vb, d.so);
            } else {
                const float sc = rstd * (T < 14 ? QSCALE : 1.f);
#pragma unroll
                for (int e = 0; e < 16; ++e) a[e] *= sc;
                u32x4 v[2];
#pragma unroll
                for (int b = 0; b < 2; ++b) {
                    const u32x4 t = pack8(a, b);
                    const auto s0 = __builtin_amdgcn_permlane32_swap(t[0], t[2], false, false), s1 = __builtin_amdgcn_permlane32_swap(t[1], t[3], false, false);
                    v[b] = (u32x4){s0[0], s1[0], s0[1], s1[1]};
                }
                const unsigned pad = T < 14 ? 0u : 0x3f80u;
                const u32x4 vx = {pad, 0u, d.sa ? v[0][2] : v[1][2], d.sa ? v[0][3] : v[1][3]};
                if (d.sa) { v[0][2] = pad; v[0][3] = 0u; }
                if (d.sb) { v[1][2] = pad; v[1][3] = 0u; }
                stc(v[0], rWs, d.va, d.so); stc(v[1], rWs, d.vb, d.so); stc(vx, rWs, d.vx, d.so);
            }
        };
        if (w < 4) {
            rdaddr();
            unsigned s1b = WS_S1 + (i * 14 + 7 * (xcd & 1)) * 2048, s1w = hh ? OOB : kc0 * 2048 + (32 * r + t32) * 16;
            PINS(s1b); PINV(s1w);
            tag += 1; PINV(tag); PINV(cpf);
            STAMP(25);
            const Dst d0 = mkdst(3 * g + p0), d1 = mkdst(3 * g + p0 + 1);
            if (g >= 9) {
                const unsigned f = (i16 & 3) == 0 ? 0x3f803f80u : 0u;
#pragma unroll
                for (int p = 0; p < 3; ++p) {
                    const unsigned tp = 3 * g + p - 28, hd = 32 * tp / 28;
                    const bool ok = 3 * g + p >= 28 && (g4 == 0 || (g4 == 1 && 32 * tp - 28 * hd >= 24));
                    stc((u32x4){f, f, f, f}, rWs, ok ? (hd + g4) * 262144 + (i16 >> 3) * 1024 + (28 + (i16 & 3) + 32 * ((i16 >> 2) & 1)) * 16 : OOB, WS_KV + rt * 4096 + 2048);
                }
            }
            unsigned cwa = LB + CSB + p0 * 1024 + t32 * 32 + r * 8, cra = LB + CSB + p0 * 1024 + t32 * 32 + hh * 16, mwa = LB + MFB + (32 * r + t32) * 16, rra = LB + RSB + (32 * r + 4 * hh) * 4, rwa = LB + RSB + (32 * r + t32) * 4;
            unsigned s1r = hh * 2048 + (32 * r + t32) * 16;
            PINV(cwa); PINV(cra); PINV(mwa); PINV(rra); PINV(rwa); PINV(s1r);
            const unsigned tl = (unsigned)tag;
            auto bodyA = [&](auto V) AI {
                f32x4 ca = {}, cb = {}, cc = {}, cd = {};
                u32x4 xf[4], w0[4], w1[4], sg[4];
                unsigned sb = 0;
                auto rd = [&](int s) AI { xf[s] = ldsr128(xa[s] + sb); w0[s] = ldstr2(wa[s] + sb, wa[s] + sb + 768); w1[s] = ldstr2(wa[s] + sb + 64, wa[s] + sb + 64 + 768); };
                auto mm = [&](int s) AI { if (V) { a0 = MFMA32(xf[s], w0[s], a0); a1 = MFMA32(xf[s], w1[s], a1); } else { a0 = MFMA32(w0[s], xf[s], a0); a1 = MFMA32(w1[s], xf[s], a1); } };
                STAMP(24);
                BAR();
                STAMP(16);
                rd(0); rd(1); rd(2); rd(3); SBAR();
                {
                    f32x4 sa = {}, sc = {}, qa = {}, qb = {};
#pragma unroll
                    for (int s = 0; s < 4; ++s) { msum(xf[s], sa, sc); msq(xf[s], qa, qb); }
                    qa += qb;
                    const float s1 = swapadd(sa[0] + sc[0]), s2 = swapadd((lane & 2) ? ((lane & 1) ? qa[3] : qa[2]) : ((lane & 1) ? qa[1] : qa[0]));
                    stc((u32x4){__float_as_uint(s1), tl, __float_as_uint(s2), tl}, rWs, s1w, s1b);
                }
                auto phase = [&]() AI {
                    msum(w0[0], ca, cb); msum(w1[0], cc, cd); SBAR();
#pragma unroll
                    for (int s = 0; s < 4; ++s) {
                        if (V) a0 = MFMA32(xf[s], w0[s], a0); else a0 = MFMA32(w0[s], xf[s], a0);
                        SBAR();
                        w0[s] = ldstr2(wa[s] + sb, wa[s] + sb + 768); SBAR();
                        if (V) a1 = MFMA32(xf[s], w1[s], a1); else a1 = MFMA32(w1[s], xf[s], a1);
                        SBAR();
                        xf[s] = ldsr128(xa[s] + sb); w1[s] = ldstr2(wa[s] + sb + 64, wa[s] + sb + 64 + 768); SBAR();
                    }
                };
#pragma nounroll
                for (int c = 1; c < 6; ++c) {
                    sb = c == 4 ? 0 : sb + SLOT;
                    BAR(); SBAR();
                    phase();
                }
                sb = 2 * SLOT;
                BAR(); SBAR();
                sg[0] = ldc(rWs, s1r, s1b); sg[1] = ldc(rWs, s1r, s1b + 4096); sg[2] = ldc(rWs, s1r, s1b + 8192); sg[3] = ldc(rWs, s1r, s1b + 10240);
                SBAR();
                phase();
                msum(w0[0], ca, cb); msum(w1[0], cc, cd); SBAR();
                mm(0); mm(1); SBAR();
                __builtin_amdgcn_s_waitcnt(0x0F70);
#pragma nounroll
                for (unsigned spin = 0; spin < SPIN_CAP; ++spin) {
                    const bool ok = sg[0][1] == tl && sg[0][3] == tl && sg[1][1] == tl && sg[1][3] == tl && sg[2][1] == tl && sg[2][3] == tl && sg[3][1] == tl && sg[3][3] == tl;
                    if (__all(ok)) break;
                    FENCE();
                    sg[0] = ldc(rWs, s1r, s1b); sg[1] = ldc(rWs, s1r, s1b + 4096); sg[2] = ldc(rWs, s1r, s1b + 8192); sg[3] = ldc(rWs, s1r, s1b + 10240);
                    __builtin_amdgcn_s_waitcnt(0x0F70);
                }
                mm(2); mm(3); SBAR();
                STAMP(1);
                const float s1 = swapadd(__uint_as_float(sg[0][0]) + __uint_as_float(sg[1][0]) + __uint_as_float(sg[2][0]) + (hh ? __uint_as_float(sg[3][0]) : 0.f));
                const float s2 = swapadd(__uint_as_float(sg[0][2]) + __uint_as_float(sg[1][2]) + __uint_as_float(sg[2][2]) + (hh ? __uint_as_float(sg[3][2]) : 0.f));
                const float mean = s1 * (1.f / 448.f), var = fmaxf(s2 * (1.f / 448.f) - mean * mean, 0.f), rstd = __builtin_amdgcn_rsqf(var + LN_EPS);
                const u32x2 m2 = split_m(mean);
                ldsw128(mwa, (u32x4){m2[0], m2[1], __float_as_uint(rstd), 0u}); ldswf(rwa, rstd);
                ldsw64(cwa, split_c(swapadd(ca[0] + cb[0]))); ldsw64(cwa + 1024, split_c(swapadd(cc[0] + cd[0])));
                const u32x4 mf = {m2[0], m2[1], m2[0], m2[1]};
                LBAR();
                STAMP(23);
                const u32x4 c0 = ldsr128(cra), c1 = ldsr128(cra + 1024);
                f32x4 rs[4];
                if (V) {
#pragma unroll
                    for (int e = 0; e < 4; ++e) rs[e] = ldsrf4(rra + 32 * e);
                }
                if (V) { a0 = MFMA32(mf, c0, a0); a1 = MFMA32(mf, c1, a1); } else { a0 = MFMA32(c0, mf, a0); a1 = MFMA32(c1, mf, a1); }
                SBAR();
                fin(V, a0, rstd, rs, 3 * g + p0, d0);
                fin(V, a1, rstd, rs, 3 * g + p0 + 1, d1);
            };
            if (isv) bodyA(std::true_type{}); else bodyA(std::false_type{});
        } else {
            const rsrc_t rX = __builtin_amdgcn_make_buffer_rsrc((void*)x, 0, 2048 * 896, 0x00020000);
            const rsrc_t rW = __builtin_amdgcn_make_buffer_rsrc((void*)wqkv, 0, 448 * 2688, 0x00020000);
            __builtin_amdgcn_s_setprio(1);
            const unsigned vx0 = (128 * i + 32 * r + (lane >> 3)) * 896 + (((lane & 7) ^ g4) << 4), vx1 = ((vx0 + 8 * 896) ^ 64) - 1024, vx2 = vx0 + 16 * 896 - 2048, vx3 = vx1 + 16 * 896 - 2048;
            unsigned kc = kc0, vw[3];
            dma4v(rX, vx0, vx1, vx2, vx3, kc * 128, LB + r * 4096);
#pragma unroll
            for (int e = 0; e < 3; ++e) { const unsigned f = e * 64 + lane, row = f / 12; vw[e] = (16 * r + row) * 2688 + 192 * g + (f - row * 12) * 16 - e * 1024; }
            dma3v(rW, vw[0], vw[1], vw[2], kc * 64 * 2688, LB + 16384 + r * 3072);
            kc = kc == 6 ? 0 : kc + 1;
            dma4v(rX, vx0, vx1, vx2, vx3, kc * 128, LB + SLOT + r * 4096);
            dma3v(rW, vw[0], vw[1], vw[2], kc * 64 * 2688, LB + SLOT + 16384 + r * 3072);
            rdaddr();
            const Dst d0 = mkdst(3 * g + p0);
            unsigned cwa = LB + CSB + p0 * 1024 + t32 * 32 + r * 8, cra = LB + CSB + p0 * 1024 + t32 * 32 + hh * 16, mra = LB + MFB + (32 * r + t32) * 16, rra = LB + RSB + (32 * r + 4 * hh) * 4;
            PINV(cwa); PINV(cra); PINV(mra); PINV(rra);
            auto bodyB = [&](auto V) AI {
                f32x4 ca = {}, cb = {};
                u32x4 xf[4], w0[4];
                unsigned sb = 0;
                auto rd = [&](int s) AI { xf[s] = ldsr128(xa[s] + sb); w0[s] = ldstr2(wa[s] + sb, wa[s] + sb + 768); };
                auto mm = [&](int s) AI { if (V) a0 = MFMA32(xf[s], w0[s], a0); else a0 = MFMA32(w0[s], xf[s], a0); };
                kc = kc == 6 ? 0 : kc + 1;
                STAMP(24);
                VBAR(7);
                STAMP(16);
                rd(0); rd(1); rd(2); rd(3); SBAR();
                dma4v(rX, vx0, vx1, vx2, vx3, kc * 128, LB + 2 * SLOT + r * 4096);
                dma3v(rW, vw[0], vw[1], vw[2], kc * 64 * 2688, LB + 2 * SLOT + 16384 + r * 3072);
#pragma nounroll
                for (int c = 1; c < 7; ++c) {
                    sb = c == 4 ? 0 : sb + SLOT;
                    kc = kc == 6 ? 0 : kc + 1;
                    const unsigned nb = LB + ((c + 2) & 3) * SLOT;
                    const rsrc_t rXc = __builtin_amdgcn_make_buffer_rsrc((void*)x, 0, c < 5 ? 2048 * 896 : 0, 0x00020000);
                    const rsrc_t rWc = __builtin_amdgcn_make_buffer_rsrc((void*)wqkv, 0, c < 5 ? 448 * 2688 : 0, 0x00020000);
                    SBAR();
                    VBAR(7); SBAR();
                    msum(w0[0], ca, cb); SBAR(); mm(0); SBAR(); rd(0); SBAR();
                    mm(1); SBAR(); rd(1); SBAR();
                    dma4v(rXc, vx0, vx1, vx2, vx3, kc * 128, nb + r * 4096); SBAR();
                    mm(2); SBAR(); rd(2); SBAR();
                    mm(3); SBAR(); rd(3); SBAR();
                    dma3v(rWc, vw[0], vw[1], vw[2], kc * 64 * 2688, nb + 16384 + r * 3072); SBAR();
                }
                __builtin_amdgcn_s_setprio(0);
                msum(w0[0], ca, cb); SBAR();
                mm(0); mm(1); mm(2); mm(3); SBAR();
                STAMP(1);
                tag += 1; PINV(tag);
                ldsw64(cwa, split_c(swapadd(ca[0] + cb[0])));
                LBAR();
                STAMP(23);
                const u32x4 c0 = ldsr128(cra), mr = ldsr128(mra);
                f32x4 rs[4];
                if (V) {
#pragma unroll
                    for (int e = 0; e < 4; ++e) rs[e] = ldsrf4(rra + 32 * e);
                }
                const u32x4 mf = {mr[0], mr[1], mr[0], mr[1]};
                if (V) a0 = MFMA32(mf, c0, a0); else a0 = MFMA32(c0, mf, a0);
                fin(V, a0, __uint_as_float(mr[2]), rs, 3 * g + p0, d0);
            };
            if (isv) bodyB(std::true_type{}); else bodyB(std::false_type{});
        }
        STAMP(2);
        DRAINBAR();
        if (w == 7) publish(ws, WS_FA + (g * 16 + i) * 128, tag, lane);
        STAMP(3);
    } else {
        const unsigned hid = (bq - 28) * 8 + xcd, u = hid + 32 * w;
        if (w < 4) {
            if (w < 2) __builtin_amdgcn_s_setprio(1);
            const bool gu = u < 112;
            const unsigned q4 = i16 >> 2, p4 = i16 & 3;
            const rsrc_t rA = __builtin_amdgcn_make_buffer_rsrc((void*)(gu ? wg : wo), 0, gu ? 448 * 3584 : 448 * 896, 0x00020000);
            const rsrc_t rB = __builtin_amdgcn_make_buffer_rsrc((void*)(gu ? wu : wo), 0, gu ? 448 * 3584 : 448 * 896, 0x00020000);
            const unsigned pitch = gu ? 3584 : 896, step = gu ? 16 * 3584 : 128;
            const unsigned va = (lane >> 3) * pitch + (lane & 7) * 16 + (gu ? 112 * (u & 3) * 3584 + (u >> 2) * 128 : (u - 112) * 25088);
            const unsigned vb = va + 8 * pitch, vc = gu ? va : va + 16 * pitch, vd = vc + 8 * pitch;
            u32x4 rv[28];
#pragma unroll
            for (int e = 0; e < 7; ++e) {
                rv[4 * e] = __builtin_amdgcn_raw_buffer_load_b128(rA, va + e * step, 0, 0); rv[4 * e + 1] = __builtin_amdgcn_raw_buffer_load_b128(rA, vb + e * step, 0, 0);
                rv[4 * e + 2] = __builtin_amdgcn_raw_buffer_load_b128(rB, vc + e * step, 0, 0); rv[4 * e + 3] = __builtin_amdgcn_raw_buffer_load_b128(rB, vd + e * step, 0, 0);
            }
            SBAR();
            STAMP(60);
            const unsigned lw = LB + w * 8192 + l16;
            const unsigned la = LB + w * 8192 + p4 * 8 + (gu ? 16 * (g4 & 1) + 8 * hh + q4 : 16 * (g4 >> 1) + 4 * (g4 & 1) + q4) * 128, lb = la + (gu ? 512 : 1024);
            const unsigned dst = gu ? WS_W4 + (4 * (u >> 2) * 29 + 7 * (u & 3)) * 1024 : WS_WO + (u - 112) * 1024, dA = gu ? 29 * 1024 : 16384, dS = gu ? 1024 : 65536;
            f32x4 cs[8] = {};
#pragma unroll
            for (int e = 0; e < 7; ++e) {
#pragma unroll
                for (int k = 0; k < 4; ++k) ldsw128(lw + (e & 1) * 4096 + k * 1024, rv[4 * e + k]);
                asm volatile("s_waitcnt lgkmcnt(0)" ::: "memory");
                if (e == 0) STAMP(61);
                if (e == 6) STAMP(62);
                u32x4 fr[4];
#pragma unroll
                for (int k = 0; k < 4; ++k) fr[k] = ldstr2(la + (e & 1) * 4096 + k * 32, lb + (e & 1) * 4096 + k * 32);
#pragma unroll
                for (int k = 0; k < 4; ++k) {
                    if (!gu && g4 == 3) { fr[k][2] = 0; fr[k][3] = 0; }
                    msum(fr[k], cs[2 * k], cs[2 * k + 1]);
                    stc(fr[k], rWs, l16, dst + e * dS + k * dA);
                }
            }
            const unsigned cv = gu && hh == 0 ? (28 - 7 * (u & 3)) * 1024 + (t32 + 32 * ((u >> 1) & 1)) * 16 + (u & 1) * 8 : OOB;
#pragma unroll
            for (int k = 0; k < 4; ++k) __builtin_amdgcn_raw_buffer_store_b64(split_c(swapadd(cs[2 * k][0] + cs[2 * k + 1][0])), rWs, cv, dst + k * 29 * 1024, SC1);
            __builtin_amdgcn_s_setprio(0);
        }
        tag += 1; PINV(cpf);
        STAMP(2);
        DRAINBAR();
        if (w == 7) publish(ws, WS_FA + (224 + hid) * 128, tag, lane);
        STAMP(3);
    }
    PINS(w);
    const unsigned i3 = 2 * xcd + (bq >> 4), j3 = bq & 15;
    const unsigned wvo = w < 7 ? l16 : OOB;
    u32x4 W[29];
    {
        const unsigned h = 2 * xcd + (bq >> 4), i = bq & 15;
        const unsigned u = w & 3, kh = w >> 2;
        unsigned kvs = WS_KV + (h * 64 + 32 * kh + u) * 4096;
        unsigned qs = WS_QF + (h * 64 + 4 * i) * 2048 + w * 1024, w4b = WS_W4 + (7 * j3 + min(w, 6u)) * 29 * 1024;
        constexpr unsigned QB = 3 * 32768;
        unsigned rb = LB + kh * 16384 + l16, qa = LB + QB + u * 2048 + l16;
        const rsrc_t rX7 = __builtin_amdgcn_make_buffer_rsrc(w == 7 ? (void*)wd : (void*)ws, 0, w == 7 ? (bid < 196 ? 1792 * 896 : 0) : WS_END, 0x00020000);
        unsigned x7v = w == 7 ? (64 * (bid % 28) + (lane >> 3)) * 896 + (bid / 28) * 128 + (lane & 7) * 16 : l16, x7s = w == 7 ? 0 : w4b + 16384, x7d = w == 7 ? 7168 : 1024;
        auto w4load = [&](int p) AI { if (p >= 16 && p < 24) W[p] = ldc(rX7, x7v, x7s + (p - 16) * x7d); else W[p] = ldc(rWs, wvo + (p & 3) * 1024, w4b + (p >> 2) * 4096); };
        PINS(kvs); PINS(qs); PINS(w4b); PINV(rb); PINV(qa); PINV(x7v); PINS(x7s); PINS(x7d);
        __builtin_amdgcn_s_waitcnt(0x0F70);
        if (w == 7) {
            GAS u64* pk = (GAS u64*)(ws + WS_FA + ((14 + 14 * hh + (28 * h + 27 * (g4 & 1)) / 32) / 3 * 16 + i16) * 128);
            GAS u64* pq = (GAS u64*)(ws + WS_FA + ((28 * h + 27 * (lane & 1)) / 32 / 3 * 16 + i) * 128);
#pragma nounroll
            for (unsigned spin = 0; spin < SPIN_CAP; ++spin) {
                const u64 a = __hip_atomic_load(pk, RLX_AGENT), b = __hip_atomic_load(pq, RLX_AGENT);
                if (__all((a == tag) & (b == tag))) break;
                __builtin_amdgcn_s_sleep(1);
            }
        }
        BAR();
        STAMP(4);
        asm volatile("s_mov_b32 m0, %0\n\ts_nop 0\n\tbuffer_load_dwordx4 %1, %2, %3 offen sc1 lds" ::"s"(LB + QB + w * 1024), "v"(l16), "s"(rWs), "s"(qs) : "memory");
        dma2c(rWs, l16, kvs - u * 2048, LB + kh * 16384 + u * 2048);
        dma2c(rWs, l16, kvs - u * 2048 + 8192, LB + kh * 16384 + 8192 + u * 2048);
        VBAR(2);
        STAMP(5);
        u32x4 q0 = ldsr128(qa), q1 = ldsr128(qa + 1024);
        const unsigned rb2 = rb + 65536, rb4 = rb + 131072;
        auto ta = [&](int t) AI { const int sl = (t >> 2) % 5, off = (t & 3) * 4096; return sl < 2 ? rb + sl * 32768 + off : sl < 4 ? rb2 + (sl - 2) * 32768 + off : rb4 + off; };
        f32x16 o = {}, S[2];
        u32x4 kf0, kf1, vf0, vf1, p0, p1 = {0u, 0u, 0u, 0u};
        u32x2 fa;
        float e14 = 0.f, e15 = 0.f;
        const f32x16 z16 = {};
        {
            kf0 = ldsr128(LB + l16); kf1 = ldsr128(LB + l16 + 1024);
            const u32x4 ka = ldsr128(ta(0)), kb = ldsr128(ta(0) + 1024);
            SBAR();
            f32x16 sl = MFMA32(kf0, q0, z16); sl = MFMA32(kf1, q1, sl);
            SBAR();
            dma4c(rWs, l16, kvs + 16384, LB + 32768 + w * 4096);
            kf0 = ldsr128(ta(1)); kf1 = ldsr128(ta(1) + 1024); vf0 = ldsr128(ta(0) + 2048); vf1 = vf0;
            float m = fmaxf(fmaxf(sl[0], sl[1]), sl[2]), m2 = fmaxf(fmaxf(sl[3], sl[4]), sl[5]);
#pragma unroll
            for (int e = 6; e < 14; e += 4) { m = fmaxf(fmaxf(m, sl[e]), sl[e + 1]); m2 = fmaxf(fmaxf(m2, sl[e + 2]), sl[e + 3]); }
            m = swapmax(fmaxf(fmaxf(fmaxf(m, sl[14]), sl[15]), m2));
            const unsigned nm = pk2(-m, 0.f);
            q0[0] = (unsigned)lane < (h & 1) * 32 ? nm : q0[0]; q1[2] = (unsigned)lane >= 32 + (h & 1) * 32 ? nm : q1[2];
            S[0] = MFMA32(ka, q0, z16); S[0] = MFMA32(kb, q1, S[0]);
            SBAR();
        }
#define EX(v) __builtin_amdgcn_exp2f(v)
        auto nD = [](int t) constexpr { return t < 4 || (t >= 6 && t < 10) || (t >= 14 && t < 18) ? 2 : 0; };
        auto nW = [](int t) constexpr { return t < 6 || t > 29 ? 0 : (t >= 10 && t < 14) || t == 18 ? 2 : 1; };
        auto cW = [nW](int t) constexpr { int c = 0; for (int k = 0; k < t; ++k) c += nW(k); return c; };
        static_assert(cW(32) == 29 && cW(18) == 16, "all gate/up fragments are fetched; slots 16.. follow the last K/V piece");
        static_for<32>([&](auto TT) AI {
            constexpr int t = TT;
            f32x16& SC = S[t & 1]; f32x16& SN = S[(t + 1) & 1];
            if (t == 0 || t == 2) VBAR(4);
            if (t == 6) {
                asm volatile("s_waitcnt vmcnt(0)" : "+v"(fa) :: "memory");
                if (w == 7 && !__all(fa[0] == (unsigned)tag && fa[1] == (unsigned)(tag >> 32))) waitf(ws, WS_FA + (224 + t32) * 128, tag);
                BAR();
            }
            if (t == 14) VBARN(cW(14) - cW(9));
            if (t == 22) VBARN(cW(22) - cW(17));
            if (t == 0 || t == 2 || t == 6 || t == 14 || t == 22) STAMP(32 + t);
            __builtin_amdgcn_s_setprio(3 - (t & 3));
            float a0_, a1_, a2_, a3_;
            u32x4 nk0, nk1, nv0, nv1;
            __builtin_amdgcn_s_waitcnt(0xC07F);
            SBAR();
            if (t < 31) SN = MFMA32(kf0, q0, z16);
            SBAR();
            nv1 = ldsr128(ta(t) + 3072);
            if (t < 30) nk0 = ldsr128(ta(t + 2));
            SBAR();
            a0_ = EX(SC[0]); a1_ = EX(SC[1]); SBAR(); p1[3] = pk2(e14, e15); SBAR(); a2_ = EX(SC[2]); a3_ = EX(SC[3]); SBAR(); p0[0] = pk2(a0_, a1_); SBAR();
            if (t > 0) o = MFMA32(vf1, p1, o);
            SBAR();
            if (t < 30) nk1 = ldsr128(ta(t + 2) + 1024);
            if (t < 31) nv0 = ldsr128(ta(t + 1) + 2048);
            SBAR();
            a0_ = EX(SC[4]); a1_ = EX(SC[5]); SBAR(); p0[1] = pk2(a2_, a3_); SBAR(); a2_ = EX(SC[6]); a3_ = EX(SC[7]); SBAR(); p0[2] = pk2(a0_, a1_); SBAR();
            if (t < 31) SN = MFMA32(kf1, q1, SN);
            SBAR();
            if constexpr (nD(t) > 0) { constexpr int stg = t < 4 ? 2 + (t >> 1) : t < 10 ? 1 + (t >> 1) : (t >> 1) - 1; dma2c(rWs, l16, kvs + stg * 16384 + (t & 1) * 2048, LB + (stg % 5) * 32768 + w * 4096 + (t & 1) * 2048); }
            if constexpr (nW(t) == 2) w4load(cW(t) + 1);
            SBAR();
            a0_ = EX(SC[8]); a1_ = EX(SC[9]); SBAR(); p0[3] = pk2(a2_, a3_); SBAR(); a2_ = EX(SC[10]); a3_ = EX(SC[11]); SBAR(); p1[0] = pk2(a0_, a1_); SBAR();
            o = MFMA32(vf0, p0, o); SBAR();
            if constexpr (nW(t) > 0) w4load(cW(t));
            if (t == 4) asm volatile("buffer_load_dwordx2 %0, %1, %2, %3 offen sc1" : "=&v"(fa) : "v"(w == 7 ? t32 * 128 : OOB), "s"(rWs), "s"(WS_FA + 224 * 128) : "memory");
            SBAR();
            a0_ = EX(SC[12]); a1_ = EX(SC[13]); SBAR(); p1[1] = pk2(a2_, a3_); SBAR(); e14 = EX(SC[14]); e15 = EX(SC[15]); SBAR(); p1[2] = pk2(a0_, a1_); SBAR();
            if (t == 5) { PINV(e14); PINV(e15); PINV(p1); }
            vf1 = nv1;
            if (t < 30) { kf0 = nk0; kf1 = nk1; }
            if (t < 31) vf0 = nv0;
        });
        __builtin_amdgcn_s_setprio(0);
        p1[3] = pk2(e14, e15); o = MFMA32(vf1, p1, o);
        STAMP(6);
        constexpr unsigned EB = 3 * 32768;
        {
            const unsigned mine = LB + EB + w * 4096, theirs = LB + EB + (w ^ 4) * 4096;
            f32x4 ka, kb, sa4, sb4;
            if (kh) { sa4 = (f32x4){o[0], o[1], o[2], o[3]}; sb4 = (f32x4){o[4], o[5], o[6], o[7]}; ka = (f32x4){o[8], o[9], o[10], o[11]}; kb = (f32x4){o[12], o[13], o[14], o[15]}; }
            else { ka = (f32x4){o[0], o[1], o[2], o[3]}; kb = (f32x4){o[4], o[5], o[6], o[7]}; sa4 = (f32x4){o[8], o[9], o[10], o[11]}; sb4 = (f32x4){o[12], o[13], o[14], o[15]}; }
            ldswf4(mine + l16, sa4); ldswf4(mine + 1024 + l16, sb4); ldswf(mine + 2048 + lane * 4, o[12]);
            unsigned ovo = (t32 >> 4) * 16384 + (i16 + 16 * hh) * 16, oso = WS_OF + ((8 * i + 2 * u) * 16 + h) * 1024 + kh * 512;
            PINV(ovo); PINS(oso);
            LBAR();
            STAMP(48);
            ka += ldsrf4(theirs + l16); kb += ldsrf4(theirs + 1024 + l16);
            float l = hi2all(o[12] + ldsrf(theirs + 2048 + lane * 4));
            float am = fmaxf(fmaxf(fmaxf(fabsf(ka[0]), fabsf(ka[1])), fmaxf(fabsf(ka[2]), fabsf(ka[3]))), fmaxf(fmaxf(fabsf(kb[0]), fabsf(kb[1])), fmaxf(fabsf(kb[2]), fabsf(kb[3]))));
            const bool good = l >= 0x1p-40f && l <= 0x1p60f && am <= 0x1p120f;
            if (__builtin_expect(!__all(good), 0)) {
                const unsigned kvb = WS_KV + h * 64 * 4096;
                float mx = -INFINITY;
#pragma nounroll
                for (unsigned t = 0; t < 64; ++t) {
                    f32x16 s = MFMA32(ldc(rWs, l16, kvb + t * 4096), q0, z16); s = MFMA32(ldc(rWs, l16, kvb + t * 4096 + 1024), q1, s);
#pragma unroll
                    for (int e = 0; e < 16; ++e) mx = fmaxf(mx, s[e]);
                }
                mx = swapmax(mx);
                o = z16;
#pragma nounroll
                for (unsigned t = 0; t < 64; ++t) {
                    f32x16 s = MFMA32(ldc(rWs, l16, kvb + t * 4096), q0, z16); s = MFMA32(ldc(rWs, l16, kvb + t * 4096 + 1024), q1, s);
#pragma unroll
                    for (int e = 0; e < 16; ++e) s[e] = __builtin_amdgcn_exp2f(s[e] - mx);
                    o = MFMA32(ldc(rWs, l16, kvb + t * 4096 + 2048), pack8(s, 0), o); o = MFMA32(ldc(rWs, l16, kvb + t * 4096 + 3072), pack8(s, 1), o);
                }
                l = hi2all(o[12]);
                if (kh) { ka = (f32x4){o[8], o[9], o[10], o[11]}; kb = (f32x4){o[12], o[13], o[14], o[15]}; }
                else { ka = (f32x4){o[0], o[1], o[2], o[3]}; kb = (f32x4){o[4], o[5], o[6], o[7]}; }
            }
            const float rl = __builtin_amdgcn_rcpf(l);
            u32x4 of = {pk2(ka[0] * rl, ka[1] * rl), pk2(ka[2] * rl, ka[3] * rl), pk2(kb[0] * rl, kb[1] * rl), pk2(kb[2] * rl, kb[3] * rl)};
            if (kh && hh) { of[2] = 0; of[3] = 0; }
            stc(of, rWs, ovo, oso);
        }
        STAMP(49);
        if (w == 7) __builtin_amdgcn_s_setprio(3);
        DRAINBAR();
        if (w == 7) publish(ws, WS_FB + (i * 16 + h) * 128, tag, lane);
        STAMP(7);
    }
    {
        const unsigned rt32 = 4 * i3 + (j3 >> 2);
        if (w < 7) {
            const unsigned ct3 = 7 * (j3 & 3) + w;
            u32x4 wf[16];
            __builtin_amdgcn_s_sleep(WOSLP);
#pragma unroll
            for (int e = 0; e < 16; ++e) wf[e] = ldc(rWs, l16 + (e & 3) * 1024, WS_WO + (ct3 * 16 + (e & ~3)) * 1024);
            const u32x2 xr0 = *(const GAS u32x2*)(x + (32 * rt32 + i16) * 896 + ct3 * 32 + g4 * 8);
            const u32x2 xr1 = *(const GAS u32x2*)(x + (32 * rt32 + 16 + i16) * 896 + ct3 * 32 + g4 * 8);
            unsigned vo = (i16 + 32 * (g4 >> 1)) * 16 + (g4 & 1) * 8, so = WS_HF + (rt32 * 28 + ct3) * 1024, sto = WS_ST + (i3 * 28 + ct3) * 1024 + (j3 >> 2) * 256;
            f32x4 d0 = {bflo(xr0[0]), bfhi(xr0[0]), bflo(xr0[1]), bfhi(xr0[1])}, d1 = {bflo(xr1[0]), bfhi(xr1[0]), bflo(xr1[1]), bfhi(xr1[1])};
            PINV(vo); PINS(so); PINS(sto); PINV(d0); PINV(d1);
            DRAINBAR();
            STAMP(8);
            u32x4 of[10];
#pragma unroll
            for (int e = 0; e < 5; ++e) { of[2 * e] = ldsr128(LB + e * 1024 + l16); of[2 * e + 1] = ldsr128(LB + 16384 + e * 1024 + l16); }
            SBAR();
#pragma unroll
            for (int e = 0; e < 16; ++e) {
                if (e == 3) BAR();
                d0 = MFMA16(wf[e], of[2 * (e % 5)], d0); d1 = MFMA16(wf[e], of[2 * (e % 5) + 1], d1);
                SBAR();
                if (e < 11) { of[2 * (e % 5)] = ldsr128(LB + (e + 5) * 1024 + l16); of[2 * (e % 5) + 1] = ldsr128(LB + 16384 + (e + 5) * 1024 + l16); }
                SBAR();
            }
            STAMP(45);
            const u32x2 h0 = {pk2(d0[0], d0[1]), pk2(d0[2], d0[3])}, h1 = {pk2(d1[0], d1[1]), pk2(d1[2], d1[3])};
            __builtin_amdgcn_raw_buffer_store_b64(h0, rWs, vo, so, SC1);
            __builtin_amdgcn_raw_buffer_store_b64(h1, rWs, vo + 256, so, SC1);
            const float a0 = bflo(h0[0]), a1 = bfhi(h0[0]), a2 = bflo(h0[1]), a3 = bfhi(h0[1]), b0 = bflo(h1[0]), b1 = bfhi(h1[0]), b2 = bflo(h1[1]), b3 = bfhi(h1[1]);
            const float sA = (a0 + a1) + (a2 + a3), sB = (a0 * a0 + a1 * a1) + (a2 * a2 + a3 * a3), sC = (b0 + b1) + (b2 + b3), sD = (b0 * b0 + b1 * b1) + (b2 * b2 + b3 * b3);
            const auto rx = __builtin_amdgcn_permlane32_swap(__float_as_uint(sA), __float_as_uint(sC), false, false);
            const auto ry = __builtin_amdgcn_permlane32_swap(__float_as_uint(sB), __float_as_uint(sD), false, false);
            const auto rz = __builtin_amdgcn_permlane16_swap(__float_as_uint(__uint_as_float(rx[0]) + __uint_as_float(rx[1])), __float_as_uint(__uint_as_float(ry[0]) + __uint_as_float(ry[1])), false, false);
            __builtin_amdgcn_raw_buffer_store_b32(__float_as_uint(__uint_as_float(rz[0]) + __uint_as_float(rz[1])), rWs, lane * 4, sto, SC1);
            DRAINBAR();
        } else {
            if (bid < 196) {
#pragma unroll
                for (int e = 0; e < 8; ++e) ldsw128(LB + 65536 + e * 1024 + l16, W[16 + e]);
                asm volatile("s_waitcnt lgkmcnt(0)" ::: "memory");
                const unsigned la = LB + 65536 + (4 * hh + (i16 >> 2)) * 128 + (g4 & 1) * 32 + (i16 & 3) * 8, dst = WS_WD + (2 * (bid / 28) * 112 + 4 * (bid % 28)) * 1024;
                u32x4 fr[8];
#pragma unroll
                for (int e = 0; e < 8; ++e) fr[e] = ldstr2(la + (e >> 1) * 2048 + (e & 1) * 64, la + (e >> 1) * 2048 + (e & 1) * 64 + 1024);
#pragma unroll
                for (int e = 0; e < 8; ++e) stc(fr[e], rWs, l16 + (e >> 1) * 1024, dst + (e & 1) * 114688);
            }
            waitf(ws, WS_FB + (i3 * 16 + i16) * 128, tag);
            STAMP(47);
#pragma unroll
            for (int e = 0; e < 8; ++e) { const int pc = (e & 1) * 4 + (e >> 1); dma4c(rWs, l16, WS_OF + rt32 * 32768 + pc * 4096, LB + pc * 4096); }
            VBAR(16);
            STAMP(8);
            VBAR(0);
            publish(ws, WS_FW + bid * 8, tag, lane);
            BAR();
            publish(ws, WS_FC + (i3 * 16 + j3) * 128, tag, lane);
        }
        STAMP(9);
    }
    u32x4 Wd[28];
    const unsigned m5 = j3 / 7, n5 = j3 % 7;
    {
        constexpr unsigned SB = 114688;
        const unsigned wdb = WS_WD + ((2 * n5 + (w & 1)) * 112 + 28 * (w >> 1)) * 1024, wdv = j3 < 14 ? l16 : OOB;
        auto wdload = [&](int p) AI { Wd[p] = ldc(rWs, wdv + (p & 3) * 1024, wdb + (p >> 2) * 4096); };
        if (w == 7) {
            unsigned hb = WS_HF + 4 * i3 * 28672;
            PINS(hb);
            waitf(ws, WS_FC + (i3 * 16 + i16) * 128, tag);
            STAMP(10);
            auto chunk = [&](int ps, int c) AI { dma4c(rWs, l16, hb + 2 * ps * 28672 + c * 4096, LB + 2 * ps * 28672 + c * 4096); dma4c(rWs, l16, hb + (2 * ps + 1) * 28672 + c * 4096, LB + (2 * ps + 1) * 28672 + c * 4096); };
            chunk(0, 0); chunk(0, 1); chunk(0, 2); chunk(0, 3);
            VBAR(16);
            chunk(0, 4); chunk(0, 5); chunk(0, 6);
            f32x4 sv[28];
            u32x4 fw0, fw1;
#pragma unroll
            for (int e = 0; e < 28; ++e) { if (e == 8) VBAR(8); sv[e] = __builtin_bit_cast(f32x4, ldc(rWs, l16 + (e & 3) * 1024, WS_ST + (i3 * 28 + (e & ~3)) * 1024)); }
            fw0 = ldc(rWs, l16, WS_FW); fw1 = ldc(rWs, l16 + 1024, WS_FW);
            FENCE();
#pragma unroll
            for (int c = 0; c < 7; ++c) chunk(1, c);
            __builtin_amdgcn_s_waitcnt(0x0F70);
            const unsigned tl = (unsigned)tag, th = (unsigned)(tag >> 32);
#pragma nounroll
            for (unsigned spin = 0; spin < SPIN_CAP; ++spin) {
                if (__all(fw0[0] == tl && fw0[1] == th && fw0[2] == tl && fw0[3] == th && fw1[0] == tl && fw1[1] == th && fw1[2] == tl && fw1[3] == th)) break;
                FENCE();
                fw0 = ldc(rWs, l16, WS_FW); fw1 = ldc(rWs, l16 + 1024, WS_FW);
                __builtin_amdgcn_s_waitcnt(0x0F70);
            }
            f32x4 sum = sv[0];
#pragma unroll
            for (int e = 1; e < 28; ++e) sum += sv[e];
            ldswf4(LB + SB + l16, sum);
            WAITBAR(0);
#pragma unroll
            for (int p = 0; p < 12; ++p) wdload(p);
            BAR();
            publish(ws, WS_FD + (i3 * 16 + j3) * 128, tag, lane);
#pragma unroll
            for (int p = 12; p < 28; ++p) wdload(p);
            if (j3 < 14) waitf(ws, WS_FD + (i3 * 16 + i16) * 128, tag);
        } else {
            STAMP(10);
            unsigned ha = LB + l16, gso = WS_GF + (4 * i3 * 112 + 7 * j3 + w) * 1024, sa = LB + SB + ((t32 >> 4) * 32 + i16) * 4;
            PINV(ha); PINS(gso); PINV(sa);
            u32x4 hf[3][2];
            BAR();
            hf[0][0] = ldsr128(ha); hf[0][1] = ldsr128(ha + 28672); hf[1][0] = ldsr128(ha + 1024); hf[1][1] = ldsr128(ha + 28672 + 1024);
            static_for<2>([&](auto PS) AI {
                constexpr int ps = PS;
                f32x16 acc[2] = {};
                float r2[2]; u32x4 mf[2];
                static_for<28>([&](auto PP) AI {
                    constexpr int p = PP, q = 28 * ps + p;
                    if (ps == 0 && (p == 6 || p == P4B)) BAR();
                    if (ps == 1 && p == 7) { __builtin_amdgcn_s_waitcnt(0x0F70); VBAR(0); }
                    if (p == 0) STAMP(33 + 2 * ps);
                    if (q == 26) ha += 57344;
#pragma unroll
                    for (int a = 0; a < 2; ++a) {
                        acc[a] = MFMA32(W[p], hf[q % 3][a], acc[a]);
                        if (q + 2 < 56) hf[(q + 2) % 3][a] = ldsr128(ha + a * 28672 + ((q + 2) % 28) * 1024);
                        SBAR();
                    }
                    if constexpr (p == 25 || p == 26) {
                        constexpr int a = p - 25;
                        const float mean = ldsrf(sa + (2 * ps + a) * 256) * (1.f / 448.f), var = fmaxf(ldsrf(sa + (2 * ps + a) * 256 + 64) * (1.f / 448.f) - mean * mean, 0.f);
                        r2[a] = __builtin_amdgcn_rcpf(var + LN_EPS); const u32x2 m2 = split_m(mean); mf[a] = (u32x4){m2[0], m2[1], m2[0], m2[1]};
                    }
                    if constexpr (ps == 0 && p >= P4B) wdload(p - P4B);
                    if constexpr (ps == 1 && p >= 7 && 28 - P4B + p - 7 < 28 - (P4B - 20)) wdload(28 - P4B + p - 7);
                    if constexpr (ps == 1 && p >= 7 && p < 7 + P4B - 20) wdload(27 - (p - 7));
                    SBAR();
                });
                acc[0] = MFMA32(W[28], mf[0], acc[0]); acc[1] = MFMA32(W[28], mf[1], acc[1]);
                if (ps == 1) STAMP(39);
#pragma unroll
                for (int a = 0; a < 2; ++a) {
                    float gv[8];
#pragma unroll
                    for (int e = 0; e < 8; ++e) gv[e] = fmaxf(acc[a][e] * r2[a], 0.f) * acc[a][e + 8];
                    stc((u32x4){pk2(gv[0], gv[1]), pk2(gv[2], gv[3]), pk2(gv[4], gv[5]), pk2(gv[6], gv[7])}, rWs, l16, gso + (2 * ps + a) * 114688);
                }
                SBAR();
            });
        }
    }
    const u32x4 hres = ldsr128(LB + (2 * (tid >> 8) + (m5 & 1)) * 28672 + (4 * n5 + ((tid & 7) >> 1)) * 1024 + (((tid >> 3) & 31) + 32 * (tid & 1)) * 16);
    STAMP(11);
    DRAINBAR();
    if (w == 7) publish(ws, WS_FD + 32768 + (i3 * 16 + j3) * 128, tag, lane);
    __builtin_amdgcn_s_setprio(0);
    STAMP(12);
    if (j3 < 14) {
        const unsigned kq = w >> 1;
        const unsigned gs = WS_GF + ((4 * i3 + m5) * 112 + 28 * kq) * 1024, ga = LB + kq * 8192 + l16, gb = ga + 65536;
        auto stage = [&](int j) AI {
            const unsigned src = gs + (j >> 2) * 2 * 114688 + (j & 3) * 8192;
            if ((j & 3) < 3) dma4c(rWs, l16, src + (w & 1) * 4096, LB + (j & 3) * 32768 + w * 4096);
            else dma2c(rWs, l16, src + (w & 1) * 2048, LB + 98304 + kq * 8192 + (w & 1) * 2048);
        };
        const unsigned rw = LB + kq * 8192 + t32 * 256;
        auto spill = [&](const f32x16& a, unsigned base) AI {
#pragma unroll
            for (int e = 0; e < 4; ++e) ldswf4(rw + base + (((8 * (w & 1) + 2 * e + hh) ^ (t32 & 15)) << 4), (f32x4){a[4 * e], a[4 * e + 1], a[4 * e + 2], a[4 * e + 3]});
        };
        stage(0); stage(1);
        f32x16 acc[2] = {};
        u32x4 gf[8];
        u32x2 fd;
        static_for<9>([&](auto JJ) AI {
            constexpr int j = JJ, n = (j & 3) < 3 ? 8 : 4, pn = ((j - 1) & 3) < 3 ? 8 : 4;
            if (j == 2) {
                asm volatile("s_waitcnt vmcnt(2)" : "+v"(fd) :: "memory");
                if (w == 7 && !__all(fd[0] == (unsigned)tag && fd[1] == (unsigned)(tag >> 32))) waitf(ws, WS_FD + 32768 + (i3 * 16 + i16) * 128, tag);
                BAR();
                STAMP(13);
            } else if (j == 6) VBAR(2); else if (j == 7) VBAR(0); else if (j < 8) VBAR(4);
            if (j == 0) STAMP(40);
            if (j == 4) STAMP(42);
            SBAR();
            const unsigned a = (j & 3) < 2 ? ga + (j & 3) * 32768 : gb + ((j & 3) - 2) * 32768;
#pragma unroll
            for (int e = 0; e < 8; ++e) {
                if (j > 0 && e < pn) acc[(j - 1) >> 2] = MFMA32(Wd[8 * ((j - 1) & 3) + e], gf[e], acc[(j - 1) >> 2]);
                SBAR();
                if (j < 8 && e < n) gf[e] = ldsr128(a + e * 1024);
                if (e == 1 && j == 1) asm volatile("buffer_load_dwordx2 %0, %1, %2, %3 offen sc1" : "=&v"(fd) : "v"(w == 7 ? i16 * 128 : OOB), "s"(rWs), "s"(WS_FD + 32768 + i3 * 2048) : "memory");
                if (e == (j == 0 ? 7 : 1) && j < 6) stage(j + 2);
                SBAR();
            }
            if (j == 5) spill(acc[0], 131072);
        });
        STAMP(14);
        spill(acc[1], 0);
        LBAR();
        const unsigned row = tid >> 3, c8 = tid & 7;
        f32x4 lo = {bflo(hres[0]), bfhi(hres[0]), bflo(hres[1]), bfhi(hres[1])}, hi = {bflo(hres[2]), bfhi(hres[2]), bflo(hres[3]), bfhi(hres[3])};
        f32x4 slo = {}, shi = {};
        const unsigned rr = LB + (row < 32 ? 131072 : 0) + (row & 31) * 256;
#pragma unroll
        for (int k = 0; k < 4; ++k) {
            slo += ldsrf4(rr + k * 8192 + (((2 * c8) ^ (row & 15)) << 4));
            shi += ldsrf4(rr + k * 8192 + (((2 * c8 + 1) ^ (row & 15)) << 4));
        }
        lo += slo; hi += shi;
        const rsrc_t rO = __builtin_amdgcn_make_buffer_rsrc(out, 0, 2048 * 896, 0x00020000);
        stc((u32x4){pk2(lo[0], lo[1]), pk2(lo[2], lo[3]), pk2(hi[0], hi[1]), pk2(hi[2], hi[3])}, rO, ((row >> 5) * 64 + (row & 31)) * 896 + c8 * 16, (128 * i3 + 32 * m5) * 896 + n5 * 128);
    } else if (bid == 255 && w == 7) {
#pragma unroll
        for (int k = 0; k < 4; ++k) waitf(ws, WS_FA + (64 * k + lane) * 128, tag);
        publish(ws, WS_E, tag, lane);
    }
}

extern "C" void kernel_launch(void* const* d_in, const int* in_sizes, int n_in, void* d_out, int out_size, void* d_ws, size_t ws_size, hipStream_t stream) {
    block_fwd<<<256, 512, 0, stream>>>((const char*)d_in[0], (const char*)d_in[1], (const char*)d_in[2], (const char*)d_in[4], (const char*)d_in[5], (const char*)d_in[6],
                                       (char*)d_out, (char*)d_ws);
}
```

```cpp
#include <hip/hip_runtime.h>
#include <stdint.h>
#include <type_traits>
#include <utility>

typedef __attribute__((ext_vector_type(8))) __bf16 bf16x8;
typedef __attribute__((ext_vector_type(2))) __bf16 bf16x2;
typedef __attribute__((ext_vector_type(2))) float f32x2;
typedef __attribute__((ext_vector_type(4))) float f32x4;
typedef __attribute__((ext_vector_type(16))) float f32x16;
typedef __attribute__((ext_vector_type(4))) unsigned u32x4;
typedef __attribute__((ext_vector_type(2))) unsigned u32x2;
typedef __attribute__((ext_vector_type(4))) short s16x4;
typedef unsigned long long u64;
typedef __amdgpu_buffer_rsrc_t rsrc_t;
#define DI __device__ __forceinline__
#define LAS __attribute__((address_space(3)))
#define GAS __attribute__((address_space(1)))
#define RLX_AGENT __ATOMIC_RELAXED, __HIP_MEMORY_SCOPE_AGENT
#define SC1 16

constexpr unsigned WS_E = 0;
constexpr unsigned WS_FA = 4096;
constexpr unsigned WS_FB = WS_FA + 32768;
constexpr unsigned WS_FC = WS_FB + 32768;
constexpr unsigned WS_FD = WS_FC + 32768;
constexpr unsigned WS_FW = WS_FD + 65536;
constexpr unsigned WS_QF = 262144;
constexpr unsigned WS_KV = WS_QF + (2u << 20);
constexpr unsigned WS_OF = WS_KV + (4u << 20);
constexpr unsigned WS_HF = WS_OF + (2u << 20);
constexpr unsigned WS_GF = WS_HF + 64u * 28 * 1024;
constexpr unsigned WS_W4 = WS_GF + 64u * 112 * 1024;
constexpr unsigned WS_WO = WS_W4 + 112u * 29 * 1024;
constexpr unsigned WS_WD = WS_WO + 28u * 16 * 1024;
constexpr unsigned WS_ST = WS_WD + 14u * 112 * 1024;
constexpr unsigned WS_S1 = WS_ST + 16u * 28 * 1024;
constexpr unsigned WS_END = WS_S1 + 16u * 14 * 2048;
constexpr unsigned OOB = 0x80000000u;

#define WOSLP 4
#define NWDA 14
#define NWDC 0
#define HDLY 24
#define ROT5 ((n5 + m5 + 2 * (i3 & 1)) % 3)
#define NWD7 28
#define CSLP 2
constexpr float QSCALE = 0.18898223650461362f * 1.4426950408889634f;
constexpr unsigned CODE_AHEAD = 41216;
constexpr float LN_EPS = 1e-5f;

__shared__ __attribute__((aligned(16))) char lds[163840];

#define MFMA32(a, b, c) __builtin_amdgcn_mfma_f32_32x32x16_bf16(__builtin_bit_cast(bf16x8, a), __builtin_bit_cast(bf16x8, b), c, 0, 0, 0)
#define MFMA16(a, b, c) __builtin_amdgcn_mfma_f32_16x16x32_bf16(__builtin_bit_cast(bf16x8, a), __builtin_bit_cast(bf16x8, b), c, 0, 0, 0)
#define SBAR() __builtin_amdgcn_sched_barrier(0)
#define WAITBAR(N) asm volatile("s_waitcnt vmcnt(" #N ") lgkmcnt(0)\n\ts_barrier" ::: "memory")
#define DRAINBAR() do { __builtin_amdgcn_s_waitcnt(0x0070); WAITBAR(0); } while (0)
#define LBAR() asm volatile("s_waitcnt lgkmcnt(0)\n\ts_barrier" ::: "memory")
#define BAR() asm volatile("s_barrier" ::: "memory")
#define VBAR(N) asm volatile("s_waitcnt vmcnt(" #N ")\n\ts_barrier" ::: "memory")
#define VBARN(N) asm volatile("s_waitcnt vmcnt(%0)\n\ts_barrier" ::"n"(N) : "memory")
#define FENCE() asm volatile("" ::: "memory")

template <int... I, class F> DI void static_for_impl(std::integer_sequence<int, I...>, F&& f) { (f(std::integral_constant<int, I>{}), ...); }
template <int N, class F> DI void static_for(F&& f) { static_for_impl(std::make_integer_sequence<int, N>{}, f); }
#define AI __attribute__((always_inline))
DI unsigned rfl(unsigned v) { return __builtin_amdgcn_readfirstlane(v); }
DI unsigned pk2(float lo, float hi) { f32x2 v = {lo, hi}; bf16x2 b = __builtin_convertvector(v, bf16x2); return __builtin_bit_cast(unsigned, b); }
DI float bflo(unsigned v) { return __uint_as_float(v << 16); }
DI float bfhi(unsigned v) { return __uint_as_float(v & 0xffff0000u); }
DI u32x4 ldsr128(unsigned a) { return *(const LAS u32x4*)(size_t)a; }
DI f32x4 ldsrf4(unsigned a) { return *(const LAS f32x4*)(size_t)a; }
DI float ldsrf(unsigned a) { return *(const LAS float*)(size_t)a; }
DI void ldsw128(unsigned a, u32x4 v) { *(LAS u32x4*)(size_t)a = v; }
DI void ldswf4(unsigned a, f32x4 v) { *(LAS f32x4*)(size_t)a = v; }
DI void ldswf(unsigned a, float v) { *(LAS float*)(size_t)a = v; }
DI void ldsw64(unsigned a, u32x2 v) { *(LAS u32x2*)(size_t)a = v; }
DI u32x2 ldstr(unsigned a) { return __builtin_bit_cast(u32x2, __builtin_amdgcn_ds_read_tr16_b64_v4i16((LAS s16x4*)(size_t)a)); }
DI u32x4 ldstr2(unsigned a, unsigned b) { u32x2 lo = ldstr(a), hi = ldstr(b); return (u32x4){lo[0], lo[1], hi[0], hi[1]}; }
DI float swapadd(float v) { auto r = __builtin_amdgcn_permlane32_swap(__float_as_uint(v), __float_as_uint(v), false, false); return __uint_as_float(r[0]) + __uint_as_float(r[1]); }
DI float swapmax(float v) { auto r = __builtin_amdgcn_permlane32_swap(__float_as_uint(v), __float_as_uint(v), false, false); return fmaxf(__uint_as_float(r[0]), __uint_as_float(r[1])); }
DI float hi2all(float v) { auto r = __builtin_amdgcn_permlane32_swap(__float_as_uint(v), __float_as_uint(v), false, false); return __uint_as_float(r[1]); }
#define MFMA4(a, b, c) __builtin_amdgcn_mfma_f32_4x4x4bf16_1k(__builtin_bit_cast(s16x4, a), __builtin_bit_cast(s16x4, b), c, 0, 0, 0)
DI void msum(u32x4 f, f32x4& c, f32x4& d) { const u32x2 one = {0x3f803f80u, 0x3f803f80u}, lo = {f[0], f[1]}, hi = {f[2], f[3]}; c = MFMA4(one, lo, c); d = MFMA4(one, hi, d); }
DI void msq(u32x4 f, f32x4& c, f32x4& d) { const u32x2 lo = {f[0], f[1]}, hi = {f[2], f[3]}; c = MFMA4(lo, lo, c); d = MFMA4(hi, hi, d); }
#define PINV(x) asm volatile("" : "+v"(x))
#define PINS(x) do { x = rfl(x); asm volatile("" : "+s"(x)); } while (0)
DI u32x4 pack8(const f32x16& a, int s) { return (u32x4){pk2(a[8 * s], a[8 * s + 1]), pk2(a[8 * s + 2], a[8 * s + 3]), pk2(a[8 * s + 4], a[8 * s + 5]), pk2(a[8 * s + 6], a[8 * s + 7])}; }
DI u32x2 split_c(float c) { const unsigned h = pk2(c, c); return (u32x2){h, pk2(c - bfhi(h), 0.f)}; }
DI u32x2 split_m(float m) { const float n = -m; const unsigned h = pk2(n, n); return (u32x2){pk2(bfhi(h), n - bfhi(h)), h & 0xffffu}; }

DI void dma4v(rsrc_t r, unsigned v0, unsigned v1, unsigned v2, unsigned v3, unsigned soff, unsigned m0v) {
    asm volatile("s_mov_b32 m0, %0\n\ts_nop 0\n\tbuffer_load_dwordx4 %1, %5, %6 offen lds\n\tbuffer_load_dwordx4 %2, %5, %6 offen offset:1024 lds\n\t"
                 "buffer_load_dwordx4 %3, %5, %6 offen offset:2048 lds\n\tbuffer_load_dwordx4 %4, %5, %6 offen offset:3072 lds" ::"s"(m0v), "v"(v0), "v"(v1), "v"(v2), "v"(v3), "s"(r), "s"(soff) : "memory");
}
DI void dma3v(rsrc_t r, unsigned v0, unsigned v1, unsigned v2, unsigned soff, unsigned m0v) {
    asm volatile("s_mov_b32 m0, %0\n\ts_nop 0\n\tbuffer_load_dwordx4 %1, %4, %5 offen lds\n\tbuffer_load_dwordx4 %2, %4, %5 offen offset:1024 lds\n\t"
                 "buffer_load_dwordx4 %3, %4, %5 offen offset:2048 lds" ::"s"(m0v), "v"(v0), "v"(v1), "v"(v2), "s"(r), "s"(soff) : "memory");
}
DI void dma2c(rsrc_t r, unsigned voff, unsigned soff, unsigned m0v) {
    asm volatile("s_mov_b32 m0, %0\n\ts_nop 0\n\tbuffer_load_dwordx4 %1, %2, %3 offen sc1 lds\n\tbuffer_load_dwordx4 %1, %2, %3 offen offset:1024 sc1 lds" ::"s"(m0v), "v"(voff), "s"(r), "s"(soff) : "memory");
}
DI void dma4c(rsrc_t r, unsigned voff, unsigned soff, unsigned m0v) {
    asm volatile("s_mov_b32 m0, %0\n\ts_nop 0\n\tbuffer_load_dwordx4 %1, %2, %3 offen sc1 lds\n\tbuffer_load_dwordx4 %1, %2, %3 offen offset:1024 sc1 lds\n\t"
                 "buffer_load_dwordx4 %1, %2, %3 offen offset:2048 sc1 lds\n\tbuffer_load_dwordx4 %1, %2, %3 offen offset:3072 sc1 lds" ::"s"(m0v), "v"(voff), "s"(r), "s"(soff) : "memory");
}
DI u32x4 ldc(rsrc_t r, unsigned voff, unsigned soff) { return __builtin_amdgcn_raw_buffer_load_b128(r, voff, soff, SC1); }
DI void stc(u32x4 v, rsrc_t r, unsigned voff, unsigned soff) { __builtin_amdgcn_raw_buffer_store_b128(v, r, voff, soff, SC1); asm volatile("s_nop 1" ::"v"(v) : "memory"); }

constexpr unsigned SPIN_CAP = 1u << 20;
DI void publish(rsrc_t r, unsigned v0, unsigned off, u64 tag) { __builtin_amdgcn_raw_buffer_store_b64(__builtin_bit_cast(u32x2, tag), r, v0, off, SC1); }
DI void waitf(rsrc_t r, unsigned voff, unsigned soff, u64 tag) {
    u64 v; unsigned n = SPIN_CAP;
    asm volatile("s_nop 4\n1:\n\tbuffer_load_dwordx2 %0, %2, %3, %4 offen sc1\n\ts_waitcnt vmcnt(0)\n\tv_cmp_ne_u64 vcc, %0, %5\n\ts_cbranch_vccz 2f\n\t"
                 "s_sub_u32 %1, %1, 1\n\ts_cbranch_scc1 2f\n\ts_sleep 1\n\ts_branch 1b\n2:"
                 : "=&v"(v), "+s"(n) : "v"(voff), "s"(r), "s"(soff), "v"(tag) : "vcc", "scc", "memory");
}
DI void waitf2(rsrc_t r, unsigned va, unsigned vb, unsigned soff, u64 tag) {
    u64 v, u; unsigned n = SPIN_CAP; u64 m;
    asm volatile("s_nop 4\n1:\n\tbuffer_load_dwordx2 %0, %4, %6, %7 offen sc1\n\tbuffer_load_dwordx2 %1, %5, %6, %7 offen sc1\n\ts_waitcnt vmcnt(0)\n\t"
                 "v_cmp_ne_u64 %3, %0, %8\n\tv_cmp_ne_u64 vcc, %1, %8\n\ts_nop 1\n\ts_or_b64 %3, %3, vcc\n\ts_cbranch_scc0 2f\n\t"
                 "s_sub_u32 %2, %2, 1\n\ts_cbranch_scc1 2f\n\ts_sleep 1\n\ts_branch 1b\n2:"
                 : "=&v"(v), "=&v"(u), "+s"(n), "=&s"(m) : "v"(va), "v"(vb), "s"(r), "s"(soff), "v"(tag) : "vcc", "scc", "memory");
}
DI float dot2(unsigned a, unsigned b, float c) { return __builtin_amdgcn_fdot2_f32_bf16(__builtin_bit_cast(bf16x2, a), __builtin_bit_cast(bf16x2, b), c, false); }

#define STAMP(n)
#define STAMPD(n)

__global__ __launch_bounds__(512, 2) void block_fwd(const char* __restrict__ x, const char* __restrict__ wqkv, const char* __restrict__ wo, const char* __restrict__ wg,
                                                    const char* __restrict__ wu, const char* __restrict__ wd, char* __restrict__ out, char* ws) {
    STAMP(0);
    const int tid = threadIdx.x, lane = tid & 63;
    unsigned w = rfl(tid >> 6);
    const unsigned bid = blockIdx.x, xcd = bid & 7, bq = bid >> 3;
    const unsigned t32 = lane & 31, hh = lane >> 5, g4 = lane >> 4, i16 = lane & 15;
    const unsigned LB = (unsigned)(size_t)(LAS char*)lds;
    const rsrc_t rWs = __builtin_amdgcn_make_buffer_rsrc(ws, 0, WS_END, 0x00020000);
    const unsigned l16 = lane * 16;
    const unsigned pv = tid == 448 ? 0 : OOB;
    u64 tag = __hip_atomic_load((GAS u64*)(ws + WS_E), RLX_AGENT);
    unsigned cpf = 0;
    if (bq >= 28 && w >= 4) {
        u64 pc; asm volatile("s_getpc_b64 %0" : "=s"(pc));
        cpf = __builtin_amdgcn_raw_buffer_load_b32(__builtin_amdgcn_make_buffer_rsrc((void*)(pc & ~127ull), 0, CODE_AHEAD, 0x00020000), lane < 22 ? (lane * 16 + (bq - 28) * 4 + w - 4) * 128 : OOB, 0, 0);
    }
    FENCE();

    if (bq < 28) {
        const unsigned i = 4 * (xcd >> 1) + bq / 7, g = 7 * (xcd & 1) + bq % 7;
        const unsigned r = w & 3, q4 = i16 >> 2, p4 = i16 & 3, rt = 4 * i + r;
        const unsigned kc0 = (g + 2 * i) % 7;
        constexpr unsigned SLOT = 28672;
        constexpr unsigned MFB = 114688, RSB = MFB + 2048, CSB = RSB + 512;
        const unsigned p0 = w < 4 ? (g == 9 ? 1 : 0) : (g == 9 ? 0 : 2);
        const bool isv = 3 * g + p0 >= 28;
        f32x16 a0 = {}, a1 = {};
        unsigned xa[4], wa[4], xb[4], wb[4];
        auto rdaddr = [&]() AI {
#pragma unroll
            for (int s = 0; s < 4; ++s) {
                const unsigned sp = (s + r) & 3;
                xa[s] = LB + (32 * r + t32) * 128 + (((2 * sp + hh) ^ ((t32 >> 1) & 7)) << 4);
                wa[s] = LB + 16384 + (16 * sp + 8 * hh + q4) * 192 + p0 * 64 + (4 * (g4 & 1) + p4) * 8;
                xb[s] = xa[s] + 2 * SLOT; wb[s] = wa[s] + 2 * SLOT;
                PINV(xb[s]); PINV(wb[s]);
            }
        };
        auto xad = [&](int c, int s) AI { return ((c & 2) ? xb[s] : xa[s]) + (c & 1) * SLOT; };
        auto wad = [&](int c, int s) AI { return ((c & 2) ? wb[s] : wa[s]) + (c & 1) * SLOT; };
        struct Dst { unsigned va, vb, vx, so; bool sa, sb; };
        auto mkdst = [&](unsigned T) AI {
            Dst d; const unsigned part = T / 14, tp = T % 14;
            if (T >= 28) { const unsigned c = 32 * tp + t32, hd = c / 28; d.va = hd * 262144 + (c - 28 * hd + 32 * hh) * 16; d.vb = d.va + 1024; d.vx = OOB; d.sa = d.sb = false; d.so = WS_KV + rt * 4096 + 2048; }
            else {
                const unsigned hs = part ? 262144 : 131072;
                const unsigned ca = 4 * tp + hh, pa = ca / 7, ra = ca - 7 * pa, cb = ca + 2, pb = cb / 7, rb = cb - 7 * pb;
                const unsigned ga = ra > 3 ? ra - 3 : ra, gb = rb > 3 ? rb - 3 : rb;
                d.va = (2 * pa + (ra > 3)) * hs + (ga >> 1) * 1024 + (t32 + 32 * (ga & 1)) * 16;
                d.vb = (2 * pb + (rb > 3)) * hs + (gb >> 1) * 1024 + (t32 + 32 * (gb & 1)) * 16;
                d.sa = ra == 3; d.sb = rb == 3;
                d.vx = ((2 * pb + 1) * hs + t32 * 16) | ((d.sa | d.sb) ? 0u : OOB);
                d.so = part ? WS_KV + rt * 4096 : WS_QF + rt * 2048;
            }
            PINV(d.va); PINV(d.vb); PINV(d.vx); PINS(d.so);
            return d;
        };
        auto fin = [&](auto V, f32x16 a, float rstd, const f32x4* rs, unsigned T, const Dst& d) AI {
            if (V) {
#pragma unroll
                for (int e = 0; e < 16; ++e) a[e] *= rs[e >> 2][e & 3];
                stc(pack8(a, 0), rWs, d.va, d.so); stc(pack8(a, 1), rWs, d.vb, d.so);
            } else {
                const float sc = rstd * (T < 14 ? QSCALE : 1.f);
#pragma unroll
                for (int e = 0; e < 16; ++e) a[e] *= sc;
                u32x4 v[2];
#pragma unroll
                for (int b = 0; b < 2; ++b) {
                    const u32x4 t = pack8(a, b);
                    const auto s0 = __builtin_amdgcn_permlane32_swap(t[0], t[2], false, false), s1 = __builtin_amdgcn_permlane32_swap(t[1], t[3], false, false);
                    v[b] = (u32x4){s0[0], s1[0], s0[1], s1[1]};
                }
                const unsigned pad = T < 14 ? 0u : 0x3f80u;
                const u32x4 vx = {pad, 0u, d.sa ? v[0][2] : v[1][2], d.sa ? v[0][3] : v[1][3]};
                if (d.sa) { v[0][2] = pad; v[0][3] = 0u; }
                if (d.sb) { v[1][2] = pad; v[1][3] = 0u; }
                stc(v[0], rWs, d.va, d.so); stc(v[1], rWs, d.vb, d.so); stc(vx, rWs, d.vx, d.so);
            }
        };
        if (w < 4) {
            rdaddr();
            unsigned s1b = WS_S1 + (i * 14 + 7 * (xcd & 1)) * 2048, s1w = hh ? OOB : kc0 * 2048 + (32 * r + t32) * 16;
            PINS(s1b); PINV(s1w);
            tag += 1; PINV(tag);
            STAMP(25);
            const Dst d0 = mkdst(3 * g + p0), d1 = mkdst(3 * g + p0 + 1);
            if (g >= 9) {
                const unsigned f = (i16 & 3) == 0 ? 0x3f803f80u : 0u, pvo = g4 * 262144 + (i16 >> 3) * 1024 + (28 + (i16 & 3) + 32 * ((i16 >> 2) & 1)) * 16;
#pragma unroll
                for (int p = 0; p < 3; ++p) {
                    const unsigned tp = 3 * g + p - 28, hd = 32 * tp / 28;
                    const unsigned nh = 3 * g + p < 28 ? 0 : 32 * tp - 28 * hd >= 24 ? 2 : 1;
                    stc((u32x4){f, f, f, f}, rWs, g4 < nh ? pvo : OOB, WS_KV + rt * 4096 + 2048 + (hd & 15) * 262144);
                }
            }
            unsigned cwa = LB + CSB + p0 * 1024 + t32 * 32 + r * 8, cra = LB + CSB + p0 * 1024 + t32 * 32 + hh * 16, mwa = LB + MFB + (32 * r + t32) * 16, rra = LB + RSB + (32 * r + 4 * hh) * 4, rwa = LB + RSB + (32 * r + t32) * 4;
            unsigned s1r = hh * 2048 + (32 * r + t32) * 16;
            PINV(cwa); PINV(cra); PINV(mwa); PINV(rra); PINV(rwa); PINV(s1r);
            const unsigned tl = (unsigned)tag;
            auto bodyA = [&](auto V) AI {
                f32x4 ca = {}, cb = {}, cc = {}, cd = {};
                u32x4 xf[4], w0[4], w1[4], sg[4];
                auto rd = [&](int s) AI { xf[s] = ldsr128(xa[s]); w0[s] = ldstr2(wa[s], wa[s] + 768); w1[s] = ldstr2(wa[s] + 64, wa[s] + 64 + 768); };
                auto mm = [&](int s) AI { if (V) { a0 = MFMA32(xf[s], w0[s], a0); a1 = MFMA32(xf[s], w1[s], a1); } else { a0 = MFMA32(w0[s], xf[s], a0); a1 = MFMA32(w1[s], xf[s], a1); } };
                STAMP(24);
                BAR();
                STAMP(16);
                rd(0); rd(1); rd(2); rd(3); SBAR();
                {
                    f32x4 sa = {}, sc = {}, qa = {}, qb = {};
#pragma unroll
                    for (int s = 0; s < 4; ++s) { msum(xf[s], sa, sc); msq(xf[s], qa, qb); }
                    qa += qb;
                    const float s1 = swapadd(sa[0] + sc[0]), s2 = swapadd((lane & 2) ? ((lane & 1) ? qa[3] : qa[2]) : ((lane & 1) ? qa[1] : qa[0]));
                    stc((u32x4){__float_as_uint(s1), tl, __float_as_uint(s2), tl}, rWs, s1w, s1b);
                }
                static_for<6>([&](auto CC) AI {
                    constexpr int c = 1 + CC;
                    BAR(); SBAR();
                    if (c == 6) { sg[0] = ldc(rWs, s1r, s1b); sg[1] = ldc(rWs, s1r, s1b + 4096); sg[2] = ldc(rWs, s1r, s1b + 8192); sg[3] = ldc(rWs, s1r, s1b + 10240); SBAR(); }
                    msum(w0[0], ca, cb); msum(w1[0], cc, cd); SBAR();
#pragma unroll
                    for (int s = 0; s < 4; ++s) {
                        if (V) a0 = MFMA32(xf[s], w0[s], a0); else a0 = MFMA32(w0[s], xf[s], a0);
                        SBAR();
                        w0[s] = ldstr2(wad(c, s), wad(c, s) + 768); SBAR();
                        if (V) a1 = MFMA32(xf[s], w1[s], a1); else a1 = MFMA32(w1[s], xf[s], a1);
                        SBAR();
                        xf[s] = ldsr128(xad(c, s)); w1[s] = ldstr2(wad(c, s) + 64, wad(c, s) + 64 + 768); SBAR();
                    }
                });
                msum(w0[0], ca, cb); msum(w1[0], cc, cd); SBAR();
                mm(0); mm(1); SBAR();
                __builtin_amdgcn_s_waitcnt(0x0F70);
#pragma nounroll
                for (unsigned spin = 0; spin < SPIN_CAP; ++spin) {
                    const bool ok = sg[0][1] == tl && sg[0][3] == tl && sg[1][1] == tl && sg[1][3] == tl && sg[2][1] == tl && sg[2][3] == tl && sg[3][1] == tl && sg[3][3] == tl;
                    if (__all(ok)) break;
                    FENCE();
                    sg[0] = ldc(rWs, s1r, s1b); sg[1] = ldc(rWs, s1r, s1b + 4096); sg[2] = ldc(rWs, s1r, s1b + 8192); sg[3] = ldc(rWs, s1r, s1b + 10240);
                    __builtin_amdgcn_s_waitcnt(0x0F70);
                }
                mm(2); mm(3); SBAR();
                STAMP(1);
                const float s1 = swapadd(__uint_as_float(sg[0][0]) + __uint_as_float(sg[1][0]) + __uint_as_float(sg[2][0]) + (hh ? __uint_as_float(sg[3][0]) : 0.f));
                const float s2 = swapadd(__uint_as_float(sg[0][2]) + __uint_as_float(sg[1][2]) + __uint_as_float(sg[2][2]) + (hh ? __uint_as_float(sg[3][2]) : 0.f));
                const float mean = s1 * (1.f / 448.f), var = fmaxf(s2 * (1.f / 448.f) - mean * mean, 0.f), rstd = __builtin_amdgcn_rsqf(var + LN_EPS);
                const u32x2 m2 = split_m(mean);
                ldsw128(mwa, (u32x4){m2[0], m2[1], __float_as_uint(rstd), 0u}); ldswf(rwa, rstd);
                ldsw64(cwa, split_c(swapadd(ca[0] + cb[0]))); ldsw64(cwa + 1024, split_c(swapadd(cc[0] + cd[0])));
                const u32x4 mf = {m2[0], m2[1], m2[0], m2[1]};
                LBAR();
                STAMP(23);
                const u32x4 c0 = ldsr128(cra), c1 = ldsr128(cra + 1024);
                f32x4 rs[4];
                if (V) {
#pragma unroll
                    for (int e = 0; e < 4; ++e) rs[e] = ldsrf4(rra + 32 * e);
                }
                if (V) { a0 = MFMA32(mf, c0, a0); a1 = MFMA32(mf, c1, a1); } else { a0 = MFMA32(c0, mf, a0); a1 = MFMA32(c1, mf, a1); }
                SBAR();
                fin(V, a0, rstd, rs, 3 * g + p0, d0);
                fin(V, a1, rstd, rs, 3 * g + p0 + 1, d1);
            };
            if (isv) bodyA(std::true_type{}); else bodyA(std::false_type{});
        } else {
            const rsrc_t rX = __builtin_amdgcn_make_buffer_rsrc((void*)x, 0, 2048 * 896, 0x00020000);
            const rsrc_t rW = __builtin_amdgcn_make_buffer_rsrc((void*)wqkv, 0, 448 * 2688, 0x00020000);
            __builtin_amdgcn_s_setprio(1);
            const unsigned vx0 = (128 * i + 32 * r + (lane >> 3)) * 896 + (((lane & 7) ^ g4) << 4), vx1 = ((vx0 + 8 * 896) ^ 64) - 1024, vx2 = vx0 + 16 * 896 - 2048, vx3 = vx1 + 16 * 896 - 2048;
            unsigned kc = kc0, vw[3];
            dma4v(rX, vx0, vx1, vx2, vx3, kc * 128, LB + r * 4096);
#pragma unroll
            for (int e = 0; e < 3; ++e) { const unsigned f = e * 64 + lane, row = f / 12; vw[e] = (16 * r + row) * 2688 + 192 * g + (f - row * 12) * 16 - e * 1024; }
            dma3v(rW, vw[0], vw[1], vw[2], kc * 64 * 2688, LB + 16384 + r * 3072);
            rdaddr();
            kc = kc == 6 ? 0 : kc + 1;
            dma4v(rX, vx0, vx1, vx2, vx3, kc * 128, LB + SLOT + r * 4096);
            dma3v(rW, vw[0], vw[1], vw[2], kc * 64 * 2688, LB + SLOT + 16384 + r * 3072);
            auto bodyB = [&](auto V) AI {
                f32x4 ca = {}, cb = {};
                u32x4 xf[4], w0[4];
                auto rd = [&](int c, int s) AI { xf[s] = ldsr128(xad(c, s)); w0[s] = ldstr2(wad(c, s), wad(c, s) + 768); };
                auto mm = [&](int s) AI { if (V) a0 = MFMA32(xf[s], w0[s], a0); else a0 = MFMA32(w0[s], xf[s], a0); };
                STAMP(24);
                static_for<7>([&](auto CC) AI {
                    constexpr int c = CC;
                    kc = kc == 6 ? 0 : kc + 1;
                    if (c < 6) VBAR(7); else VBAR(0);
                    if (c == 0) STAMP(16);
                    SBAR();
                    if (c > 0) { msum(w0[0], ca, cb); SBAR(); mm(0); SBAR(); }
                    rd(c, 0); SBAR();
                    if (c > 0) { mm(1); SBAR(); }
                    rd(c, 1); SBAR();
                    if (c > 0 && c < 5) { dma4v(rX, vx0, vx1, vx2, vx3, kc * 128, LB + ((c + 2) & 3) * SLOT + r * 4096); SBAR(); }
                    if (c > 0) { mm(2); SBAR(); }
                    rd(c, 2); SBAR();
                    if (c > 0) { mm(3); SBAR(); }
                    rd(c, 3); SBAR();
                    if (c == 0) dma4v(rX, vx0, vx1, vx2, vx3, kc * 128, LB + 2 * SLOT + r * 4096);
                    if (c < 5) { dma3v(rW, vw[0], vw[1], vw[2], kc * 64 * 2688, LB + ((c + 2) & 3) * SLOT + 16384 + r * 3072); SBAR(); }
                });
                __builtin_amdgcn_s_setprio(0);
                msum(w0[0], ca, cb); SBAR();
                mm(0); mm(1); mm(2); mm(3); SBAR();
                STAMP(1);
                tag += 1; PINV(tag);
                ldsw64(LB + CSB + p0 * 1024 + t32 * 32 + r * 8, split_c(swapadd(ca[0] + cb[0])));
                const Dst d0 = mkdst(3 * g + p0);
                unsigned cra = LB + CSB + p0 * 1024 + t32 * 32 + hh * 16, mra = LB + MFB + (32 * r + t32) * 16, rra = LB + RSB + (32 * r + 4 * hh) * 4;
                PINV(cra); PINV(mra); PINV(rra);
                LBAR();
                STAMP(23);
                const u32x4 c0 = ldsr128(cra), mr = ldsr128(mra);
                f32x4 rs[4];
                if (V) {
#pragma unroll
                    for (int e = 0; e < 4; ++e) rs[e] = ldsrf4(rra + 32 * e);
                }
                const u32x4 mf = {mr[0], mr[1], mr[0], mr[1]};
                if (V) a0 = MFMA32(mf, c0, a0); else a0 = MFMA32(c0, mf, a0);
                fin(V, a0, __uint_as_float(mr[2]), rs, 3 * g + p0, d0);
            };
            if (isv) bodyB(std::true_type{}); else bodyB(std::false_type{});
        }
        STAMP(2);
        DRAINBAR();
        if (w == 7) publish(rWs, pv, WS_FA + (g * 16 + i) * 128, tag);
        STAMP(3);
    } else {
        const unsigned hid = (bq - 28) * 8 + xcd, u = hid + 32 * w;
        if (w < 4) {
            if (w < 2) __builtin_amdgcn_s_setprio(1);
            __builtin_amdgcn_s_sleep(HDLY);
            const bool gu = u < 112;
            const unsigned q4 = i16 >> 2, p4 = i16 & 3;
            const rsrc_t rA = __builtin_amdgcn_make_buffer_rsrc((void*)(gu ? wg : wo), 0, gu ? 448 * 3584 : 448 * 896, 0x00020000);
            const rsrc_t rB = __builtin_amdgcn_make_buffer_rsrc((void*)(gu ? wu : wo), 0, gu ? 448 * 3584 : 448 * 896, 0x00020000);
            const unsigned pitch = gu ? 3584 : 896, step = gu ? 16 * 3584 : 128;
            const unsigned va = (lane >> 3) * pitch + (lane & 7) * 16 + (gu ? 112 * (u & 3) * 3584 + (u >> 2) * 128 : (u - 112) * 25088);
            const unsigned vb = va + 8 * pitch, vc = gu ? va : va + 16 * pitch, vd = vc + 8 * pitch;
            u32x4 rv[28];
#pragma unroll
            for (int e = 0; e < 7; ++e) {
                rv[4 * e] = __builtin_amdgcn_raw_buffer_load_b128(rA, va + e * step, 0, 0); rv[4 * e + 1] = __builtin_amdgcn_raw_buffer_load_b128(rA, vb + e * step, 0, 0);
                rv[4 * e + 2] = __builtin_amdgcn_raw_buffer_load_b128(rB, vc + e * step, 0, 0); rv[4 * e + 3] = __builtin_amdgcn_raw_buffer_load_b128(rB, vd + e * step, 0, 0);
            }
            SBAR();
            STAMP(60);
            const unsigned lw = LB + w * 8192 + l16;
            const unsigned la = LB + w * 8192 + p4 * 8 + (gu ? 16 * (g4 & 1) + 8 * hh + q4 : 16 * (g4 >> 1) + 4 * (g4 & 1) + q4) * 128, lb = la + (gu ? 512 : 1024);
            const unsigned dst = gu ? WS_W4 + (4 * (u >> 2) * 29 + 7 * (u & 3)) * 1024 : WS_WO + (u - 112) * 1024, dA = gu ? 29 * 1024 : 16384, dS = gu ? 1024 : 65536;
            f32x4 cs[8] = {};
#pragma unroll
            for (int e = 0; e < 7; ++e) {
#pragma unroll
                for (int k = 0; k < 4; ++k) ldsw128(lw + (e & 1) * 4096 + k * 1024, rv[4 * e + k]);
                asm volatile("s_waitcnt lgkmcnt(0)" ::: "memory");
                if (e == 0) STAMP(61);
                if (e == 6) STAMP(62);
                u32x4 fr[4];
#pragma unroll
                for (int k = 0; k < 4; ++k) fr[k] = ldstr2(la + (e & 1) * 4096 + k * 32, lb + (e & 1) * 4096 + k * 32);
#pragma unroll
                for (int k = 0; k < 4; ++k) {
                    if (!gu && g4 == 3) { fr[k][2] = 0; fr[k][3] = 0; }
                    msum(fr[k], cs[2 * k], cs[2 * k + 1]);
                    stc(fr[k], rWs, l16, dst + e * dS + k * dA);
                }
            }
            const unsigned cv = ((28 - 7 * (u & 3)) * 1024 + (t32 + 32 * ((u >> 1) & 1)) * 16 + (u & 1) * 8) | ((gu & (hh == 0)) ? 0u : OOB);
#pragma unroll
            for (int k = 0; k < 4; ++k) __builtin_amdgcn_raw_buffer_store_b64(split_c(swapadd(cs[2 * k][0] + cs[2 * k + 1][0])), rWs, cv, dst + k * 29 * 1024, SC1);
            __builtin_amdgcn_s_setprio(0);
        }
        tag += 1; PINV(cpf);
        STAMP(2);
        DRAINBAR();
        if (w == 7) publish(rWs, pv, WS_FA + (224 + hid) * 128, tag);
        STAMP(3);
    }
    PINS(w);
    const unsigned i3 = 2 * xcd + (bq >> 4), j3 = bq & 15;
    const unsigned wvo = w < 7 ? l16 : OOB;
    u32x4 W[29], wf[16];
    u32x2 xr0, xr1;
    const unsigned rt32 = 4 * i3 + (j3 >> 2), ct3 = 7 * (j3 & 3) + min(w, 6u), rot3 = j3 >> 2;
    auto woload = [&](int e) AI { wf[e] = ldc(rWs, wvo + (e & 3) * 1024, WS_WO + ct3 * 16384 + (((e >> 2) + rot3) & 3) * 4096); };
    {
        const unsigned h = 2 * xcd + (bq >> 4), i = bq & 15;
        const unsigned u = w & 3, kh = w >> 2;
        unsigned kvs = WS_KV + (h * 64 + 32 * kh + u) * 4096;
        unsigned qs = WS_QF + (h * 64 + 4 * i) * 2048 + w * 1024, w4b = WS_W4 + (7 * j3 + min(w, 6u)) * 29 * 1024;
        constexpr unsigned QB = 3 * 32768;
        unsigned rb = LB + kh * 16384 + l16, qa = LB + QB + u * 2048 + l16;
        auto w4load = [&](int p) AI { W[p] = ldc(rWs, wvo + (p & 3) * 1024, w4b + (p >> 2) * 4096); };
        unsigned dv = bid < 196 ? (64 * (bid % 28) + 16 * (w >> 1) + (lane >> 2)) * 896 + (bid / 28) * 128 + (w & 1) * 64 + (lane & 3) * 16 : OOB;
        u32x4 dw;
        constexpr unsigned EB = 3 * 32768;
        unsigned xw = LB + EB + w * 4096 + l16, ovo = (t32 >> 4) * 16384 + (i16 + 16 * hh) * 16, oso = WS_OF + ((8 * i + 2 * u) * 16 + h) * 1024 + kh * 512, fbo = WS_FB + (i * 16 + h) * 128;
        unsigned dso = WS_WD + ((2 * (bid / 28) + (w & 1)) * 112 + 4 * (bid % 28) + (w >> 1)) * 1024;
        PINV(xw); PINV(ovo); PINS(oso); PINS(fbo); PINS(dso);
        PINS(kvs); PINS(qs); PINS(w4b); PINV(rb); PINV(qa); PINV(dv);
        __builtin_amdgcn_s_waitcnt(0x0F70);
        if (w == 7) waitf2(rWs, ((14 + 14 * hh + (28 * h + 27 * (g4 & 1)) / 32) / 3 * 16 + i16) * 128, ((28 * h + 27 * (lane & 1)) / 32 / 3 * 16 + i) * 128, WS_FA, tag);
        BAR();
        STAMP(4);
        asm volatile("s_mov_b32 m0, %0\n\ts_nop 0\n\tbuffer_load_dwordx4 %1, %2, %3 offen sc1 lds" ::"s"(LB + QB + w * 1024), "v"(l16), "s"(rWs), "s"(qs) : "memory");
        dma2c(rWs, l16, kvs - u * 2048, LB + kh * 16384 + u * 2048);
        dma2c(rWs, l16, kvs - u * 2048 + 8192, LB + kh * 16384 + 8192 + u * 2048);
        VBAR(2);
        STAMP(5);
        u32x4 q0 = ldsr128(qa), q1 = ldsr128(qa + 1024);
        unsigned rb2 = rb + 65536, rb4 = rb + 131072;
        PINV(rb2); PINV(rb4);
        auto ta = [&](int t) AI { const int sl = (t >> 2) % 5, off = (t & 3) * 4096; return sl < 2 ? rb + sl * 32768 + off : sl < 4 ? rb2 + (sl - 2) * 32768 + off : rb4 + off; };
        f32x16 o = {}, S[2];
        u32x4 kf0, kf1, vf0, vf1, p0, p1 = {0u, 0u, 0u, 0u};
        u32x2 fa;
        float e14 = 0.f, e15 = 0.f;
        const f32x16 z16 = {};
        {
            kf0 = ldsr128(LB + l16); kf1 = ldsr128(LB + l16 + 1024);
            const u32x4 ka = ldsr128(ta(0)), kb = ldsr128(ta(0) + 1024);
            SBAR();
            f32x16 sl = MFMA32(kf0, q0, z16); sl = MFMA32(kf1, q1, sl);
            SBAR();
            dma4c(rWs, l16, kvs + 16384, LB + 32768 + w * 4096);
            kf0 = ldsr128(ta(1)); kf1 = ldsr128(ta(1) + 1024); vf0 = ldsr128(ta(0) + 2048); vf1 = vf0;
            float m = fmaxf(fmaxf(sl[0], sl[1]), sl[2]), m2 = fmaxf(fmaxf(sl[3], sl[4]), sl[5]);
#pragma unroll
            for (int e = 6; e < 14; e += 4) { m = fmaxf(fmaxf(m, sl[e]), sl[e + 1]); m2 = fmaxf(fmaxf(m2, sl[e + 2]), sl[e + 3]); }
            m = swapmax(fmaxf(fmaxf(fmaxf(m, sl[14]), sl[15]), m2));
            const unsigned nm = pk2(-m, 0.f);
            q0[0] = (unsigned)lane < (h & 1) * 32 ? nm : q0[0]; q1[2] = (unsigned)lane >= 32 + (h & 1) * 32 ? nm : q1[2];
            S[0] = MFMA32(ka, q0, z16); S[0] = MFMA32(kb, q1, S[0]);
            SBAR();
        }
#define EX(v) __builtin_amdgcn_exp2f(v)
        auto nD = [](int t) constexpr { return t < 4 || (t >= 6 && t < 10) || (t >= 14 && t < 18) ? 2 : 0; };
        auto nW = [](int t) constexpr { return t < 6 || t > 29 ? 0 : (t >= 10 && t < 14) || t == 18 ? 2 : 1; };
        auto cW = [nW](int t) constexpr { int c = 0; for (int k = 0; k < t; ++k) c += nW(k); return c; };
        static_assert(cW(32) == 29, "all gate/up fragments are fetched");
        static_for<32>([&](auto TT) AI {
            constexpr int t = TT;
            f32x16& SC = S[t & 1]; f32x16& SN = S[(t + 1) & 1];
            if (t == 0 || t == 2) VBAR(4);
            if (t == 6) {
                asm volatile("s_waitcnt vmcnt(0)" : "+v"(fa) :: "memory");
                if (w == 7 && !__all(fa[0] == (unsigned)tag && fa[1] == (unsigned)(tag >> 32))) waitf(rWs, t32 * 128, WS_FA + 224 * 128, tag);
                BAR();
            }
            if (t == 14) VBARN(cW(14) - cW(9) + 1);
            if (t == 22) VBARN(cW(22) - cW(17));
            if (t == 0 || t == 2 || t == 6 || t == 14 || t == 22) STAMP(32 + t);
            __builtin_amdgcn_s_setprio(3 - (t & 3));
            float a0_, a1_, a2_, a3_;
            u32x4 nk0, nk1, nv0, nv1;
            __builtin_amdgcn_s_waitcnt(0xC07F);
            SBAR();
            if (t < 31) SN = MFMA32(kf0, q0, z16);
            SBAR();
            nv1 = ldsr128(ta(t) + 3072);
            if (t < 30) nk0 = ldsr128(ta(t + 2));
            SBAR();
            a0_ = EX(SC[0]); a1_ = EX(SC[1]); SBAR(); p1[3] = pk2(e14, e15); SBAR(); a2_ = EX(SC[2]); a3_ = EX(SC[3]); SBAR(); p0[0] = pk2(a0_, a1_); SBAR();
            if (t > 0) o = MFMA32(vf1, p1, o);
            SBAR();
            if (t < 30) nk1 = ldsr128(ta(t + 2) + 1024);
            if (t < 31) nv0 = ldsr128(ta(t + 1) + 2048);
            SBAR();
            a0_ = EX(SC[4]); a1_ = EX(SC[5]); SBAR(); p0[1] = pk2(a2_, a3_); SBAR(); a2_ = EX(SC[6]); a3_ = EX(SC[7]); SBAR(); p0[2] = pk2(a0_, a1_); SBAR();
            if (t < 31) SN = MFMA32(kf1, q1, SN);
            SBAR();
            if constexpr (nD(t) > 0) { constexpr int stg = t < 4 ? 2 + (t >> 1) : t < 10 ? 1 + (t >> 1) : (t >> 1) - 1; dma2c(rWs, l16, kvs + stg * 16384 + (t & 1) * 2048, LB + (stg % 5) * 32768 + w * 4096 + (t & 1) * 2048); }
            if constexpr (nW(t) == 2) w4load(cW(t) + 1);
            if constexpr (t >= 23 && t < 27) woload(t - 23);
            if constexpr (t == 27) xr0 = *(const GAS u32x2*)(x + (32 * rt32 + i16) * 896 + ct3 * 32 + g4 * 8);
            if constexpr (t == 28) xr1 = *(const GAS u32x2*)(x + (32 * rt32 + 16 + i16) * 896 + ct3 * 32 + g4 * 8);
            SBAR();
            a0_ = EX(SC[8]); a1_ = EX(SC[9]); SBAR(); p0[3] = pk2(a2_, a3_); SBAR(); a2_ = EX(SC[10]); a3_ = EX(SC[11]); SBAR(); p1[0] = pk2(a0_, a1_); SBAR();
            o = MFMA32(vf0, p0, o); SBAR();
            if constexpr (nW(t) > 0) w4load(cW(t));
            if constexpr (t == 9) dw = __builtin_amdgcn_raw_buffer_load_b128(__builtin_amdgcn_make_buffer_rsrc((void*)wd, 0, 1792 * 896, 0x00020000), dv, 0, 0);
            if constexpr (t == 22) ldsw128(xw + 3072, dw);
            if constexpr (t == 23) { const unsigned a = LB + EB + 3072 + w * 4096 + (4 * hh + (i16 >> 2)) * 64 + (g4 & 1) * 32 + (i16 & 3) * 8; dw = ldstr2(a, a + 512); }
            if constexpr (t == 24) stc(dw, rWs, bid < 196 ? l16 : OOB, dso);
            if (t == 4) asm volatile("buffer_load_dwordx2 %0, %1, %2, %3 offen sc1" : "=&v"(fa) : "v"(w == 7 ? t32 * 128 : OOB), "s"(rWs), "s"(WS_FA + 224 * 128) : "memory");
            SBAR();
            a0_ = EX(SC[12]); a1_ = EX(SC[13]); SBAR(); p1[1] = pk2(a2_, a3_); SBAR(); e14 = EX(SC[14]); e15 = EX(SC[15]); SBAR(); p1[2] = pk2(a0_, a1_); SBAR();
            if (t == 5) { PINV(e14); PINV(e15); PINV(p1); }
            vf1 = nv1;
            if (t < 30) { kf0 = nk0; kf1 = nk1; }
            if (t < 31) vf0 = nv0;
        });
        __builtin_amdgcn_s_setprio(0);
        p1[3] = pk2(e14, e15); o = MFMA32(vf1, p1, o);
        STAMP(6);
        {
            f32x4 ka, kb, sa4, sb4;
            if (kh) { sa4 = (f32x4){o[0], o[1], o[2], o[3]}; sb4 = (f32x4){o[4], o[5], o[6], o[7]}; ka = (f32x4){o[8], o[9], o[10], o[11]}; kb = (f32x4){o[12], o[13], o[14], o[15]}; }
            else { ka = (f32x4){o[0], o[1], o[2], o[3]}; kb = (f32x4){o[4], o[5], o[6], o[7]}; sa4 = (f32x4){o[8], o[9], o[10], o[11]}; sb4 = (f32x4){o[12], o[13], o[14], o[15]}; }
            ldswf4(xw, sa4); ldswf4(xw + 1024, sb4); ldswf(xw + 2048, o[12]);
            const unsigned xr = xw ^ 16384;
            LBAR();
            STAMP(48);
            ka += ldsrf4(xr); kb += ldsrf4(xr + 1024);
            float l = hi2all(o[12] + ldsrf(xr + 2048));
            float am = fmaxf(fmaxf(fmaxf(fabsf(ka[0]), fabsf(ka[1])), fmaxf(fabsf(ka[2]), fabsf(ka[3]))), fmaxf(fmaxf(fabsf(kb[0]), fabsf(kb[1])), fmaxf(fabsf(kb[2]), fabsf(kb[3]))));
            const bool good = l >= 0x1p-40f && l <= 0x1p60f && am <= 0x1p120f;
            if (__builtin_expect(!__all(good), 0)) {
                const unsigned kvb = WS_KV + h * 64 * 4096;
                float mx = -INFINITY;
#pragma nounroll
                for (unsigned t = 0; t < 64; ++t) {
                    f32x16 s = MFMA32(ldc(rWs, l16, kvb + t * 4096), q0, z16); s = MFMA32(ldc(rWs, l16, kvb + t * 4096 + 1024), q1, s);
#pragma unroll
                    for (int e = 0; e < 16; ++e) mx = fmaxf(mx, s[e]);
                }
                mx = swapmax(mx);
                o = z16;
#pragma nounroll
                for (unsigned t = 0; t < 64; ++t) {
                    f32x16 s = MFMA32(ldc(rWs, l16, kvb + t * 4096), q0, z16); s = MFMA32(ldc(rWs, l16, kvb + t * 4096 + 1024), q1, s);
#pragma unroll
                    for (int e = 0; e < 16; ++e) s[e] = __builtin_amdgcn_exp2f(s[e] - mx);
                    o = MFMA32(ldc(rWs, l16, kvb + t * 4096 + 2048), pack8(s, 0), o); o = MFMA32(ldc(rWs, l16, kvb + t * 4096 + 3072), pack8(s, 1), o);
                }
                l = hi2all(o[12]);
                if (kh) { ka = (f32x4){o[8], o[9], o[10], o[11]}; kb = (f32x4){o[12], o[13], o[14], o[15]}; }
                else { ka = (f32x4){o[0], o[1], o[2], o[3]}; kb = (f32x4){o[4], o[5], o[6], o[7]}; }
            }
            const float rl = __builtin_amdgcn_rcpf(l);
            u32x4 of = {pk2(ka[0] * rl, ka[1] * rl), pk2(ka[2] * rl, ka[3] * rl), pk2(kb[0] * rl, kb[1] * rl), pk2(kb[2] * rl, kb[3] * rl)};
            if (kh && hh) { of[2] = 0; of[3] = 0; }
            stc(of, rWs, ovo, oso);
        }
        STAMP(49);
        if (w == 7) __builtin_amdgcn_s_setprio(3);
        DRAINBAR();
        if (w == 7) { publish(rWs, pv, fbo, tag); publish(rWs, pv, WS_FW + bid * 8, tag); }
        STAMP(7);
    }
    u32x4 Wd[28], hres;
    const unsigned m5 = j3 / 7, n5 = j3 % 7;
    const unsigned gs0 = WS_GF + (4 * i3 + m5) * 114688;
    const unsigned rot5 = ROT5;
    auto gko = [&](int j) AI { unsigned c = j + rot5; c = c >= 3 ? c - 3 : c; return j == 3 ? 24576u : c * 8192; };
    const unsigned hra = LB + (2 * (tid >> 8) + (m5 & 1)) * 28672 + (4 * n5 + ((tid & 7) >> 1)) * 1024 + (((tid >> 3) & 31) + 32 * (tid & 1)) * 16;
    const unsigned wdb = WS_WD + ((2 * n5 + (w & 1)) * 112 + 28 * (w >> 1)) * 1024, wdv = j3 < 14 ? l16 : OOB;
    auto wdload = [&](int p) AI { Wd[p] = ldc(rWs, wdv + (p & 3) * 1024, wdb + gko(p >> 3) + ((p >> 2) & 1) * 4096); };
    {
        constexpr unsigned PSB = 122880;
        constexpr unsigned SB = 114688;
        if (w == 7) {
            {
            waitf(rWs, i16 * 128, WS_FB + i3 * 2048, tag);
            STAMP(47);
#pragma unroll
            for (int e = 0; e < 8; ++e) dma4c(rWs, l16, WS_OF + rt32 * 32768 + (e & 1) * 16384 + (((e >> 1) + rot3) & 3) * 4096, LB + (e & 1) * 16384 + (e >> 1) * 4096);
            VBAR(16);
            STAMP(8);
            u32x4 fw0 = ldc(rWs, l16, WS_FW), fw1 = ldc(rWs, l16 + 1024, WS_FW);
            VBAR(2);
            __builtin_amdgcn_s_waitcnt(0x0F70);
            const unsigned tl = (unsigned)tag, th = (unsigned)(tag >> 32);
#pragma nounroll
            for (unsigned spin = 0; spin < SPIN_CAP; ++spin) {
                if (__all(fw0[0] == tl && fw0[1] == th && fw0[2] == tl && fw0[3] == th && fw1[0] == tl && fw1[1] == th && fw1[2] == tl && fw1[3] == th)) break;
                FENCE();
                fw0 = ldc(rWs, l16, WS_FW); fw1 = ldc(rWs, l16 + 1024, WS_FW);
                __builtin_amdgcn_s_waitcnt(0x0F70);
            }
#pragma unroll
            for (int p = 0; p < NWD7; ++p) wdload(p);
            BAR();
            publish(rWs, pv, WS_FC + (i3 * 16 + j3) * 128, tag);
            }
            STAMP(9);
            unsigned hb = WS_HF + 4 * i3 * 28672;
            PINS(hb);
            waitf(rWs, i16 * 128, WS_FC + i3 * 2048, tag);
            STAMP(10);
            auto chunk = [&](int ps, unsigned c) AI { dma4c(rWs, l16, hb + 2 * ps * 28672 + c * 4096, LB + 2 * ps * 28672 + c * 4096); dma4c(rWs, l16, hb + (2 * ps + 1) * 28672 + c * 4096, LB + (2 * ps + 1) * 28672 + c * 4096); };
            chunk(0, 0); chunk(0, 1);
            unsigned c = 2 + j3 % 5;
            chunk(0, c); c = c == 6 ? 2 : c + 1; chunk(0, c);
            VBAR(16);
#pragma unroll
            for (int k = 0; k < 3; ++k) { c = c == 6 ? 2 : c + 1; chunk(0, c); }
            chunk(1, 0);
            VBAR(8);
            __builtin_amdgcn_s_setprio(0);
            chunk(1, 1);
            u32x4 sv[8];
            const unsigned tl = (unsigned)tag;
#pragma nounroll
            for (unsigned spin = 0; spin < SPIN_CAP; ++spin) {
                bool ok = true;
                FENCE();
#pragma unroll
                for (int e = 0; e < 8; ++e) sv[e] = ldc(rWs, l16 + (e & 3) * 1024, WS_ST + i3 * 8192 + (e >> 2) * 4096);
#pragma unroll
                for (int e = 0; e < 8; ++e) ok &= (sv[e][1] == tl) & (sv[e][3] == tl);
                if (__all(ok)) break;
            }
            __builtin_amdgcn_s_waitcnt(0x0F70);
#pragma unroll
            for (int e = 0; e < 2; ++e)
                ldsw64(LB + SB + e * 512 + lane * 8, (u32x2){__float_as_uint((__uint_as_float(sv[e][0]) + __uint_as_float(sv[e + 2][0])) + (__uint_as_float(sv[e + 4][0]) + __uint_as_float(sv[e + 6][0]))),
                                                            __float_as_uint((__uint_as_float(sv[e][2]) + __uint_as_float(sv[e + 2][2])) + (__uint_as_float(sv[e + 4][2]) + __uint_as_float(sv[e + 6][2])))});
#pragma unroll
            for (int k = 0; k < 3; ++k) { c = c == 6 ? 2 : c + 1; chunk(1, c); }
            LBAR();
#pragma unroll
            for (int k = 0; k < 2; ++k) { c = c == 6 ? 2 : c + 1; chunk(1, c); }
            VBAR(0);
            publish(rWs, pv, WS_FD + (i3 * 16 + j3) * 128, tag);
            hres = ldsr128(hra);
#pragma unroll
            for (int p = NWD7; p < 28; ++p) wdload(p);
            if (j3 < 14) {
                waitf(rWs, i16 * 128, WS_FD + i3 * 2048, tag);
#pragma unroll
                for (int e = 0; e < 16; ++e) dma4c(rWs, l16, gs0 + ((e >> 1) & 3) * 28672 + gko(e >> 3) + (e & 1) * 4096, LB + (e < 8 ? 131072 : 0) + (e & 7) * 4096);
            }
            __builtin_amdgcn_s_setprio(3);
        } else {
            {
            __builtin_amdgcn_s_sleep(WOSLP);
#pragma unroll
            for (int e = 4; e < 16; ++e) woload(e);
            unsigned vo = (i16 + 32 * (g4 >> 1)) * 16 + (g4 & 1) * 8, so = WS_HF + (rt32 * 28 + ct3) * 1024, sto = LB + PSB + w * 256 + lane * 4;
            f32x4 d0 = {bflo(xr0[0]), bfhi(xr0[0]), bflo(xr0[1]), bfhi(xr0[1])}, d1 = {bflo(xr1[0]), bfhi(xr1[0]), bflo(xr1[1]), bfhi(xr1[1])};
            PINV(vo); PINS(so); PINV(sto); PINV(d0); PINV(d1);
            DRAINBAR();
            STAMP(8);
            u32x4 of[10];
#pragma unroll
            for (int e = 0; e < 5; ++e) { of[2 * e] = ldsr128(LB + e * 1024 + l16); of[2 * e + 1] = ldsr128(LB + 16384 + e * 1024 + l16); }
            SBAR();
#pragma unroll
            for (int e = 0; e < 16; ++e) {
                if (e == 3) BAR();
                d0 = MFMA16(wf[e], of[2 * (e % 5)], d0); d1 = MFMA16(wf[e], of[2 * (e % 5) + 1], d1);
                SBAR();
                if (e < 11) { of[2 * (e % 5)] = ldsr128(LB + (e + 5) * 1024 + l16); of[2 * (e % 5) + 1] = ldsr128(LB + 16384 + (e + 5) * 1024 + l16); }
                SBAR();
            }
            STAMP(45);
            const u32x2 h0 = {pk2(d0[0], d0[1]), pk2(d0[2], d0[3])}, h1 = {pk2(d1[0], d1[1]), pk2(d1[2], d1[3])};
            __builtin_amdgcn_raw_buffer_store_b64(h0, rWs, vo, so, SC1);
            __builtin_amdgcn_raw_buffer_store_b64(h1, rWs, vo + 256, so, SC1);
            const unsigned a0 = h0[0], a1 = h0[1], b0 = h1[0], b1 = h1[1];
            const float sA = dot2(a0, 0x3f803f80u, dot2(a1, 0x3f803f80u, 0.f)), sB = dot2(a0, a0, dot2(a1, a1, 0.f)), sC = dot2(b0, 0x3f803f80u, dot2(b1, 0x3f803f80u, 0.f)), sD = dot2(b0, b0, dot2(b1, b1, 0.f));
            const auto rx = __builtin_amdgcn_permlane32_swap(__float_as_uint(sA), __float_as_uint(sC), false, false);
            const auto ry = __builtin_amdgcn_permlane32_swap(__float_as_uint(sB), __float_as_uint(sD), false, false);
            const auto rz = __builtin_amdgcn_permlane16_swap(__float_as_uint(__uint_as_float(rx[0]) + __uint_as_float(rx[1])), __float_as_uint(__uint_as_float(ry[0]) + __uint_as_float(ry[1])), false, false);
            ldswf(sto, __uint_as_float(rz[0]) + __uint_as_float(rz[1]));
            DRAINBAR();
            if (w == 6) {
                float t = 0.f;
#pragma unroll
                for (int e = 0; e < 7; ++e) t += ldsrf(LB + PSB + e * 256 + lane * 4);
                __builtin_amdgcn_raw_buffer_store_b64((u32x2){__float_as_uint(t), (unsigned)tag}, rWs, lane * 8, WS_ST + i3 * 8192 + (j3 & 3) * 2048 + (j3 >> 2) * 512, SC1);
            }
            }
            STAMP(9);
            STAMP(10);
            unsigned ha = LB + l16, gso = WS_GF + (4 * i3 * 112 + 7 * j3 + w) * 1024, sa = LB + SB + ((t32 >> 4) * 32 + i16) * 4;
            PINV(ha); PINS(gso); PINV(sa);
            u32x4 hf[3][2];
            __builtin_amdgcn_s_sleep(CSLP);
#pragma unroll
            for (int p = 0; p < NWDC; ++p) wdload(p);
            BAR();
            hf[0][0] = ldsr128(ha); hf[0][1] = ldsr128(ha + 28672); hf[1][0] = ldsr128(ha + 1024); hf[1][1] = ldsr128(ha + 28672 + 1024);
            static_for<2>([&](auto PS) AI {
                constexpr int ps = PS;
                f32x16 acc[2] = {};
                float r2[2]; u32x4 mf[2];
                static_for<28>([&](auto PP) AI {
                    constexpr int p = PP, q = 28 * ps + p;
                    if (ps == 0 && (p == 6 || p == 25)) BAR();
                    if (q == 30) hres = ldsr128(hra);
                    if (q == 40) { const u32x4 t = ldsr128(hra); hres = w < 4 ? hres : t; }
                    if (ps == 1 && p == 6) { __builtin_amdgcn_s_waitcnt(0x0F70); VBAR(0); }
                    if (p == 0) STAMP(33 + 2 * ps);
                    if (q == 6) STAMP(50); if (q == 25) STAMP(51); if (q == 34) STAMP(53); if (q == 45) STAMP(55);
                    if (q == 26) ha += 57344;
                    if ((q & 1) == 0) __builtin_amdgcn_s_setprio(2 - (q >> 1) % 3);
#pragma unroll
                    for (int a = 0; a < 2; ++a) {
                        acc[a] = MFMA32(W[p], hf[q % 3][a], acc[a]);
                        if (q + 2 < 56) hf[(q + 2) % 3][a] = ldsr128(ha + a * 28672 + ((q + 2) % 28) * 1024);
                        SBAR();
                    }
                    if constexpr (p == 25 || p == 26) {
                        constexpr int a = p - 25;
                        const float mean = ldsrf(sa + (2 * ps + a) * 256) * (1.f / 448.f), var = fmaxf(ldsrf(sa + (2 * ps + a) * 256 + 64) * (1.f / 448.f) - mean * mean, 0.f);
                        r2[a] = __builtin_amdgcn_rcpf(var + LN_EPS); const u32x2 m2 = split_m(mean); mf[a] = (u32x4){m2[0], m2[1], m2[0], m2[1]};
                    }
                    if constexpr (ps == 0 && p >= 28 - 2 * (NWDA - NWDC) && (p & 1) == 0) wdload(NWDC + (p - (28 - 2 * (NWDA - NWDC))) / 2);
                    if constexpr (ps == 1 && p >= 6 && NWDA + p - 6 < 28) wdload(NWDA + p - 6);
                    SBAR();
                });
                acc[0] = MFMA32(W[28], mf[0], acc[0]); acc[1] = MFMA32(W[28], mf[1], acc[1]);
                if (ps == 1) STAMP(39); else STAMP(52);
#pragma unroll
                for (int a = 0; a < 2; ++a) {
                    float gv[8];
#pragma unroll
                    for (int e = 0; e < 8; ++e) gv[e] = fmaxf(acc[a][e] * r2[a], 0.f) * acc[a][e + 8];
                    stc((u32x4){pk2(gv[0], gv[1]), pk2(gv[2], gv[3]), pk2(gv[4], gv[5]), pk2(gv[6], gv[7])}, rWs, l16, gso + (2 * ps + a) * 114688);
                }
                SBAR();
            });
        }
    }
    STAMP(11);
    const unsigned kq = w >> 1, row = tid >> 3, c8 = tid & 7;
    unsigned gs = gs0 + kq * 28672, ga = LB + kq * 8192 + l16, gb = ga + 65536, gc = ga + 131072, fdo = WS_FD + 32768 + i3 * 2048;
    PINS(gs); PINV(ga); PINV(gb); PINV(gc); PINS(fdo);
    DRAINBAR();
    if (w == 7) publish(rWs, pv, fdo + j3 * 128, tag);
    __builtin_amdgcn_s_setprio(0);
    STAMP(12);
    if (j3 < 14) {
        auto slot = [](int j) constexpr { return j % 5 == 0 ? 131072 : (j % 5 - 1) * 32768; };
        auto stage = [&](int j) AI {
            const unsigned src = gs + (j >> 2) * 2 * 114688 + gko(j & 3);
            if ((j & 3) < 3) dma4c(rWs, l16, src + (w & 1) * 4096, LB + slot(j) + w * 4096);
            else dma2c(rWs, l16, src + (w & 1) * 2048, LB + slot(j) + kq * 8192 + (w & 1) * 2048);
        };
        f32x16 acc[2] = {};
        u32x4 gf[8];
        auto gadr = [&](int j) AI { return slot(j) < 65536 ? ga + slot(j) : slot(j) < 131072 ? gb + (slot(j) - 65536) : gc; };
        auto rdg = [&](int j, int e) AI { gf[e] = ldsr128(gadr(j) + e * 1024); };
        auto mmg = [&](int j, int e) AI { acc[j >> 2] = MFMA32(Wd[8 * (j & 3) + e], gf[e], acc[j >> 2]); };
#pragma unroll
        for (int e = 0; e < 8; ++e) rdg(0, e);
        SBAR();
        stage(2);
        unsigned rw[4], ra, rb, ovo, oso;
        static_for<8>([&](auto JJ) AI {
            constexpr int j = 1 + JJ, n = (j & 3) < 3 ? 8 : 4, pn = ((j - 1) & 3) < 3 ? 8 : 4;
            if (j == 2) {
                static_for<8>([&](auto E) AI { mmg(1, E); });
                SBAR();
                STAMP(56);
                if (w == 7) waitf(rWs, i16 * 128, fdo, tag);
#pragma unroll
                for (int e = 0; e < 4; ++e) { rw[e] = LB + 65536 + kq * 8192 + t32 * 256 + (((8 * (w & 1) + 2 * e + hh) ^ (t32 & 15)) << 4); PINV(rw[e]); }
                VBAR(0);
                STAMP(13);
                stage(4);
            }
            if (j == 4) { VBAR(8); STAMP(42); }
            if (j == 5) VBAR(6);
            if (j == 6) VBAR(0);
            if (j == 5) STAMP(58); if (j == 6) STAMP(59);
            SBAR();
#pragma unroll
            for (int e = 0; e < 8; ++e) {
                if (j != 2 && e < pn) mmg(j - 1, e);
                SBAR();
                if (j < 8 && e < n) rdg(j, e);
                if (j == 1 && e == 3) stage(3);
                if (j == 3 && e == 1) stage(5);
                if (j == 3 && e == 7) stage(6);
                if (j == 4 && e == 7) stage(7);
                SBAR();
            }
            if (j == 5 || j == 6) {
                if ((w < 4) == (j == 5)) {
#pragma unroll
                    for (int e = 0; e < 4; ++e) ldswf4(rw[e], (f32x4){acc[0][4 * e], acc[0][4 * e + 1], acc[0][4 * e + 2], acc[0][4 * e + 3]});
                }
            }
            if (j == 4) {
                ra = LB + 65536 + (row >> 5) * 32768 + (row & 31) * 256 + (((2 * c8) ^ (row & 15)) << 4); rb = ra ^ 16;
                ovo = ((row >> 5) * 64 + (row & 31)) * 896 + c8 * 16; oso = (128 * i3 + 32 * m5) * 896 + n5 * 128;
                PINV(ra); PINV(rb); PINV(ovo); PINS(oso);
            }
        });
        STAMP(14);
#pragma unroll
        for (int e = 0; e < 4; ++e) ldswf4(rw[e] + 32768, (f32x4){acc[1][4 * e], acc[1][4 * e + 1], acc[1][4 * e + 2], acc[1][4 * e + 3]});
        f32x4 lo = {bflo(hres[0]), bfhi(hres[0]), bflo(hres[1]), bfhi(hres[1])}, hi = {bflo(hres[2]), bfhi(hres[2]), bflo(hres[3]), bfhi(hres[3])};
        PINV(lo); PINV(hi);
        LBAR();
        f32x4 pl[4], ph[4];
#pragma unroll
        for (int k = 0; k < 4; ++k) { pl[k] = ldsrf4(ra + k * 8192); ph[k] = ldsrf4(rb + k * 8192); }
        lo += (pl[0] + pl[1]) + (pl[2] + pl[3]); hi += (ph[0] + ph[1]) + (ph[2] + ph[3]);
        stc((u32x4){pk2(lo[0], lo[1]), pk2(lo[2], lo[3]), pk2(hi[0], hi[1]), pk2(hi[2], hi[3])}, __builtin_amdgcn_make_buffer_rsrc(out, 0, 2048 * 896, 0x00020000), ovo, oso);
    } else if (bid == 255 && w == 7) {
#pragma unroll
        for (int k = 0; k < 4; ++k) waitf(rWs, lane * 128, WS_FA + k * 8192, tag);
        publish(rWs, pv, WS_E, tag);
    }
}

extern "C" void kernel_launch(void* const* d_in, const int* in_sizes, int n_in, void* d_out, int out_size, void* d_ws, size_t ws_size, hipStream_t stream) {
    block_fwd<<<256, 512, 0, stream>>>((const char*)d_in[0], (const char*)d_in[1], (const char*)d_in[2], (const char*)d_in[4], (const char*)d_in[5], (const char*)d_in[6],
                                       (char*)d_out, (char*)d_ws);
}
```

```cpp
#include <hip/hip_runtime.h>
#include <stdint.h>
#include <type_traits>
#include <utility>

typedef __attribute__((ext_vector_type(8))) __bf16 bf16x8;
typedef __attribute__((ext_vector_type(2))) __bf16 bf16x2;
typedef __attribute__((ext_vector_type(2))) float f32x2;
typedef __attribute__((ext_vector_type(4))) float f32x4;
typedef __attribute__((ext_vector_type(16))) float f32x16;
typedef __attribute__((ext_vector_type(4))) unsigned u32x4;
typedef __attribute__((ext_vector_type(2))) unsigned u32x2;
typedef __attribute__((ext_vector_type(4))) short s16x4;
typedef unsigned long long u64;
typedef __attribute__((ext_vector_type(8))) unsigned long long u64x8;
typedef __amdgpu_buffer_rsrc_t rsrc_t;
#define DI __device__ __forceinline__
#define LAS __attribute__((address_space(3)))
#define GAS __attribute__((address_space(1)))
#define RLX_AGENT __ATOMIC_RELAXED, __HIP_MEMORY_SCOPE_AGENT
#define SC1 16

constexpr unsigned WS_E = 0;
constexpr unsigned WS_FA = 4096;
constexpr unsigned WS_FB = WS_FA + 32768;
constexpr unsigned WS_FC = WS_FB + 32768;
constexpr unsigned WS_FD = WS_FC + 32768;
constexpr unsigned WS_FW = WS_FD + 65536;
constexpr unsigned WS_QF = 262144;
constexpr unsigned WS_KV = WS_QF + (2u << 20);
constexpr unsigned WS_OF = WS_KV + (4u << 20);
constexpr unsigned WS_HF = WS_OF + (2u << 20);
constexpr unsigned WS_GF = WS_HF + 64u * 28 * 1024;
constexpr unsigned WS_W4 = WS_GF + 64u * 112 * 1024;
constexpr unsigned WS_WO = WS_W4 + 112u * 29 * 1024;
constexpr unsigned WS_WD = WS_WO + 28u * 16 * 1024;
constexpr unsigned WS_ST = WS_WD + 14u * 112 * 1024;
constexpr unsigned WS_S1 = WS_ST + 16u * 28 * 1024;
constexpr unsigned WS_END = WS_S1 + 16u * 14 * 2048;
constexpr unsigned OOB = 0x80000000u;

constexpr float QSCALE = 0.18898223650461362f * 1.4426950408889634f;
constexpr unsigned CODE_AHEAD = 41216;
constexpr float LN_EPS = 1e-5f;

__shared__ __attribute__((aligned(16))) char lds[163840];

#define MFMA32(a, b, c) __builtin_amdgcn_mfma_f32_32x32x16_bf16(__builtin_bit_cast(bf16x8, a), __builtin_bit_cast(bf16x8, b), c, 0, 0, 0)
#define MFMA16(a, b, c) __builtin_amdgcn_mfma_f32_16x16x32_bf16(__builtin_bit_cast(bf16x8, a), __builtin_bit_cast(bf16x8, b), c, 0, 0, 0)
#define MFMA16K(a, b, c) __builtin_amdgcn_mfma_f32_16x16x16bf16_1k(__builtin_bit_cast(s16x4, a), __builtin_bit_cast(s16x4, b), c, 0, 0, 0)
#define SBAR() __builtin_amdgcn_sched_barrier(0)
#define WAITBAR(N) asm volatile("s_waitcnt vmcnt(" #N ") lgkmcnt(0)\n\ts_barrier" ::: "memory")
#define DRAINBAR() do { __builtin_amdgcn_s_waitcnt(0x0070); WAITBAR(0); } while (0)
#define LBAR() asm volatile("s_waitcnt lgkmcnt(0)\n\ts_barrier" ::: "memory")
#define BAR() asm volatile("s_barrier" ::: "memory")
#define VBAR(N) asm volatile("s_waitcnt vmcnt(" #N ")\n\ts_barrier" ::: "memory")
#define VBARN(N) asm volatile("s_waitcnt vmcnt(%0)\n\ts_barrier" ::"n"(N) : "memory")
#define FENCE() asm volatile("" ::: "memory")

template <int... I, class F> DI void static_for_impl(std::integer_sequence<int, I...>, F&& f) { (f(std::integral_constant<int, I>{}), ...); }
template <int N, class F> DI void static_for(F&& f) { static_for_impl(std::make_integer_sequence<int, N>{}, f); }
#define AI __attribute__((always_inline))
DI unsigned rfl(unsigned v) { return __builtin_amdgcn_readfirstlane(v); }
DI unsigned pk2(float lo, float hi) { f32x2 v = {lo, hi}; bf16x2 b = __builtin_convertvector(v, bf16x2); return __builtin_bit_cast(unsigned, b); }
DI float bflo(unsigned v) { return __uint_as_float(v << 16); }
DI float bfhi(unsigned v) { return __uint_as_float(v & 0xffff0000u); }
DI u32x4 ldsr128(unsigned a) { return *(const LAS u32x4*)(size_t)a; }
DI f32x4 ldsrf4(unsigned a) { return *(const LAS f32x4*)(size_t)a; }
DI float ldsrf(unsigned a) { return *(const LAS float*)(size_t)a; }
DI void ldsw128(unsigned a, u32x4 v) { *(LAS u32x4*)(size_t)a = v; }
DI void ldswf4(unsigned a, f32x4 v) { *(LAS f32x4*)(size_t)a = v; }
DI void ldswf(unsigned a, float v) { *(LAS float*)(size_t)a = v; }
DI void ldsw64(unsigned a, u32x2 v) { *(LAS u32x2*)(size_t)a = v; }
DI u32x2 ldstr(unsigned a) { return __builtin_bit_cast(u32x2, __builtin_amdgcn_ds_read_tr16_b64_v4i16((LAS s16x4*)(size_t)a)); }
DI u32x4 ldstr2(unsigned a, unsigned b) { u32x2 lo = ldstr(a), hi = ldstr(b); return (u32x4){lo[0], lo[1], hi[0], hi[1]}; }
DI float swapadd(float v) { auto r = __builtin_amdgcn_permlane32_swap(__float_as_uint(v), __float_as_uint(v), false, false); return __uint_as_float(r[0]) + __uint_as_float(r[1]); }
DI float swapmax(float v) { auto r = __builtin_amdgcn_permlane32_swap(__float_as_uint(v), __float_as_uint(v), false, false); return fmaxf(__uint_as_float(r[0]), __uint_as_float(r[1])); }
DI float hi2all(float v) { auto r = __builtin_amdgcn_permlane32_swap(__float_as_uint(v), __float_as_uint(v), false, false); return __uint_as_float(r[1]); }
#define MFMA4(a, b, c) __builtin_amdgcn_mfma_f32_4x4x4bf16_1k(__builtin_bit_cast(s16x4, a), __builtin_bit_cast(s16x4, b), c, 0, 0, 0)
DI void msum(u32x4 f, f32x4& c, f32x4& d) { const u32x2 one = {0x3f803f80u, 0x3f803f80u}, lo = {f[0], f[1]}, hi = {f[2], f[3]}; c = MFMA4(one, lo, c); d = MFMA4(one, hi, d); }
DI void msq(u32x4 f, f32x4& c, f32x4& d) { const u32x2 lo = {f[0], f[1]}, hi = {f[2], f[3]}; c = MFMA4(lo, lo, c); d = MFMA4(hi, hi, d); }
#define PINV(x) asm volatile("" : "+v"(x))
#define PINS(x) do { x = rfl(x); asm volatile("" : "+s"(x)); } while (0)
DI u32x4 pack8(const f32x16& a, int s) { return (u32x4){pk2(a[8 * s], a[8 * s + 1]), pk2(a[8 * s + 2], a[8 * s + 3]), pk2(a[8 * s + 4], a[8 * s + 5]), pk2(a[8 * s + 6], a[8 * s + 7])}; }
DI u32x2 split_c(float c) { const unsigned h = pk2(c, c); return (u32x2){h, pk2(c - bfhi(h), 0.f)}; }
DI u32x2 split_m(float m) { const float n = -m; const unsigned h = pk2(n, n); return (u32x2){pk2(bfhi(h), n - bfhi(h)), h & 0xffffu}; }

DI void dma4v(rsrc_t r, unsigned v0, unsigned v1, unsigned v2, unsigned v3, unsigned soff, unsigned m0v) {
    asm volatile("s_mov_b32 m0, %0\n\ts_nop 0\n\tbuffer_load_dwordx4 %1, %5, %6 offen lds\n\tbuffer_load_dwordx4 %2, %5, %6 offen offset:1024 lds\n\t"
                 "buffer_load_dwordx4 %3, %5, %6 offen offset:2048 lds\n\tbuffer_load_dwordx4 %4, %5, %6 offen offset:3072 lds" ::"s"(m0v), "v"(v0), "v"(v1), "v"(v2), "v"(v3), "s"(r), "s"(soff) : "memory");
}
DI void dma3v(rsrc_t r, unsigned v0, unsigned v1, unsigned v2, unsigned soff, unsigned m0v) {
    asm volatile("s_mov_b32 m0, %0\n\ts_nop 0\n\tbuffer_load_dwordx4 %1, %4, %5 offen lds\n\tbuffer_load_dwordx4 %2, %4, %5 offen offset:1024 lds\n\t"
                 "buffer_load_dwordx4 %3, %4, %5 offen offset:2048 lds" ::"s"(m0v), "v"(v0), "v"(v1), "v"(v2), "s"(r), "s"(soff) : "memory");
}
DI void dma2v(rsrc_t r, unsigned v0, unsigned v1, unsigned soff, unsigned m0v) {
    asm volatile("s_mov_b32 m0, %0\n\ts_nop 0\n\tbuffer_load_dwordx4 %1, %3, %4 offen lds\n\tbuffer_load_dwordx4 %2, %3, %4 offen offset:1024 lds" ::"s"(m0v), "v"(v0), "v"(v1), "s"(r), "s"(soff) : "memory");
}
DI void dma2c(rsrc_t r, unsigned voff, unsigned soff, unsigned m0v) {
    asm volatile("s_mov_b32 m0, %0\n\ts_nop 0\n\tbuffer_load_dwordx4 %1, %2, %3 offen sc1 lds\n\tbuffer_load_dwordx4 %1, %2, %3 offen offset:1024 sc1 lds" ::"s"(m0v), "v"(voff), "s"(r), "s"(soff) : "memory");
}
DI void dma4c(rsrc_t r, unsigned voff, unsigned soff, unsigned m0v) {
    asm volatile("s_mov_b32 m0, %0\n\ts_nop 0\n\tbuffer_load_dwordx4 %1, %2, %3 offen sc1 lds\n\tbuffer_load_dwordx4 %1, %2, %3 offen offset:1024 sc1 lds\n\t"
                 "buffer_load_dwordx4 %1, %2, %3 offen offset:2048 sc1 lds\n\tbuffer_load_dwordx4 %1, %2, %3 offen offset:3072 sc1 lds" ::"s"(m0v), "v"(voff), "s"(r), "s"(soff) : "memory");
}
DI u32x4 ldc(rsrc_t r, unsigned voff, unsigned soff) { return __builtin_amdgcn_raw_buffer_load_b128(r, voff, soff, SC1); }
DI void stc(u32x4 v, rsrc_t r, unsigned voff, unsigned soff) { __builtin_amdgcn_raw_buffer_store_b128(v, r, voff, soff, SC1); asm volatile("s_nop 1" ::"v"(v) : "memory"); }

constexpr unsigned SPIN_CAP = 1u << 18;
constexpr u64 TAG_STEP = 0x9E3779B97F4A7C15ull;
DI void publish(rsrc_t r, unsigned v0, unsigned off, u64 tag) { __builtin_amdgcn_raw_buffer_store_b64(__builtin_bit_cast(u32x2, tag), r, v0, off, SC1); }
DI void waitf(rsrc_t r, unsigned voff, unsigned soff, u64 tag) {
    u64 v; unsigned n = SPIN_CAP;
    asm volatile("s_nop 4\n1:\n\tbuffer_load_dwordx2 %0, %2, %3, %4 offen sc1\n\ts_waitcnt vmcnt(0)\n\tv_cmp_ne_u64 vcc, %0, %5\n\ts_cbranch_vccz 2f\n\t"
                 "s_sub_u32 %1, %1, 1\n\ts_cbranch_scc1 2f\n\ts_sleep 1\n\ts_branch 1b\n2:"
                 : "=&v"(v), "+s"(n) : "v"(voff), "s"(r), "s"(soff), "v"(tag) : "vcc", "scc", "memory");
}
DI void arrive16(rsrc_t r, unsigned voff, unsigned soff) { asm volatile("s_mov_b64 exec, 0xffff\n\ts_nop 3\n\tbuffer_atomic_add %0, %1, %2, %3 offen\n\ts_mov_b64 exec, -1" ::"v"(1u), "v"(voff), "s"(r), "s"(soff) : "memory"); }
DI void waitc(rsrc_t r, unsigned soff) {
    unsigned v, n = SPIN_CAP;
    asm volatile("s_nop 4\n1:\n\tbuffer_load_dword %0, off, %2, %3 sc1\n\ts_waitcnt vmcnt(0)\n\tv_cmp_ne_u32 vcc, 16, %0\n\ts_cbranch_vccz 2f\n\t"
                 "s_sub_u32 %1, %1, 1\n\ts_cbranch_scc1 2f\n\ts_sleep 1\n\ts_branch 1b\n2:"
                 : "=&v"(v), "+s"(n) : "s"(r), "s"(soff) : "vcc", "scc", "memory");
}
DI float dot2(unsigned a, unsigned b, float c) { return __builtin_amdgcn_fdot2_f32_bf16(__builtin_bit_cast(bf16x2, a), __builtin_bit_cast(bf16x2, b), c, false); }

__global__ __launch_bounds__(512, 2) void block_fwd(const char* x_, const char* wqkv_, const char* wo_, const char* wg_, const char* wu_, const char* wd_, char* out_, char* ws_) {
    u64x8 ka;
    asm volatile("s_load_dwordx16 %0, %1, 0x0" : "=s"(ka) : "s"(__builtin_amdgcn_kernarg_segment_ptr()));
    const int tid = threadIdx.x, lane = tid & 63;
    unsigned w = rfl(tid >> 6);
    const unsigned bid = blockIdx.x, xcd = bid & 7, bq = bid >> 3;
    const unsigned b1 = (bq + 28) & 31;
    const unsigned t32 = lane & 31, hh = lane >> 5, g4 = lane >> 4, i16 = lane & 15;
    const unsigned LB = (unsigned)(size_t)(LAS char*)lds;
    const unsigned l16 = lane * 16;
    unsigned cpf = 0;
    if (b1 >= 28 && w >= 4) {
        u64 pc; asm volatile("s_getpc_b64 %0" : "=s"(pc));
        cpf = __builtin_amdgcn_raw_buffer_load_b32(__builtin_amdgcn_make_buffer_rsrc((void*)(pc & ~127ull), 0, CODE_AHEAD, 0x00020000), lane < 22 ? (lane * 16 + (b1 - 28) * 4 + w - 4) * 128 : OOB, 0, 0);
    }
    const unsigned i1 = 4 * (xcd >> 1) + b1 / 7, g = 7 * (xcd & 1) + b1 % 7;
    unsigned kc0 = (g + 2 * i1) % 7;
    constexpr unsigned SLOT = 28672;
    unsigned vx0 = (128 * i1 + 32 * (w & 3) + (lane >> 3)) * 896 + (((lane & 7) ^ g4) << 4), vx1 = ((vx0 + 8 * 896) ^ 64) - 1024, vx2 = vx0 + 16 * 896 - 2048, vx3 = vx1 + 16 * 896 - 2048, vw[3];
    PINS(kc0); PINV(vx0); PINV(vx1); PINV(vx2); PINV(vx3);
    asm volatile("s_waitcnt lgkmcnt(0)" : "+s"(ka));
    const char *x = (const char*)ka[0], *wqkv = (const char*)ka[1], *wo = (const char*)ka[2], *wg = (const char*)ka[3], *wu = (const char*)ka[4], *wd = (const char*)ka[5];
    char *out = (char*)ka[6], *ws = (char*)ka[7];
    const rsrc_t rWs = __builtin_amdgcn_make_buffer_rsrc(ws, 0, WS_END, 0x00020000);
    const rsrc_t rX = __builtin_amdgcn_make_buffer_rsrc((void*)x, 0, 2048 * 896, 0x00020000);
    const rsrc_t rW = __builtin_amdgcn_make_buffer_rsrc((void*)wqkv, 0, 448 * 2688, 0x00020000);
    if (b1 < 28 && w >= 4) {
        dma4v(rX, vx0, vx1, vx2, vx3, kc0 * 128, LB + (w & 3) * 4096);
#pragma unroll
        for (int e = 0; e < 3; ++e) { const unsigned f = e * 64 + lane, row = f / 12; vw[e] = (16 * (w & 3) + row) * 2688 + 192 * g + (f - row * 12) * 16 - e * 1024; }
        dma3v(rW, vw[0], vw[1], vw[2], kc0 * 64 * 2688, LB + 16384 + (w & 3) * 3072);
    }
    const unsigned u = (b1 - 28) * 8 + xcd + 32 * (w & 3), e0 = w < 4 ? 0 : 3, off4 = w < 4 ? OOB : 0;
    const bool gu = u < 112;
    if (b1 >= 28) {
        const rsrc_t rA = __builtin_amdgcn_make_buffer_rsrc((void*)(gu ? wg : wo), 0, gu ? 448 * 3584 : 448 * 896, 0x00020000);
        const rsrc_t rB = __builtin_amdgcn_make_buffer_rsrc((void*)(gu ? wu : wo), 0, gu ? 448 * 3584 : 448 * 896, 0x00020000);
        const unsigned pitch = gu ? 3584 : 896;
        unsigned va = (lane >> 3) * pitch + (lane & 7) * 16 + (gu ? 16 * (u >> 2) * 3584 + (u & 3) * 896 : (u - 112) * 25088) + e0 * 128, vc = gu ? va : va + 16 * pitch;
#pragma unroll
        for (int e = 0; e < 4; ++e) {
            if (e == 3) { va |= off4; vc |= off4; }
            dma2v(rA, va, va + 8 * pitch - 1024, e * 128, LB + w * 16384 + e * 4096);
            dma2v(rB, vc, vc + 8 * pitch - 1024, e * 128, LB + w * 16384 + e * 4096 + 2048);
        }
    }
    FENCE();
    const unsigned pv = tid == 448 ? 0 : OOB;
    u64 tag = __hip_atomic_load((GAS u64*)(ws + WS_E), RLX_AGENT);
    const unsigned i3 = 2 * xcd + (bq >> 4), j3 = bq & 15;
    const unsigned i4 = 4 * (xcd >> 1) + (bq >> 3), j4 = 8 * (xcd & 1) + (bq & 7);
    if (w == 3) __builtin_amdgcn_raw_buffer_store_b32(0u, rWs, lane < 4 ? lane * 32768 + (lane == 1 ? i4 * 16 + j4 : i3 * 16 + j3) * 128 : OOB, WS_FB, SC1);
    FENCE();

    if (b1 >= 28) {
        {
            const unsigned q4 = i16 >> 2, p4 = i16 & 3;
            BAR();
            const unsigned la = LB + w * 16384 + p4 * 8 + (gu ? 16 * (g4 & 1) + 8 * hh + q4 : 16 * (g4 >> 1) + 4 * (g4 & 1) + q4) * 128, lb = la + (gu ? 512 : 1024);
            const unsigned dA = gu ? 28 * 1024 : 16384, dst = (gu ? WS_W4 + (28 * (u & 3) * 28 + (u >> 2)) * 1024 : WS_WO + (u - 112) * 1024) + e0 * 4 * dA;
#pragma unroll
            for (int e = 0; e < 4; ++e) {
                asm volatile("s_waitcnt vmcnt(12)" ::: "memory");
                if (e == 3) { tag += TAG_STEP; PINV(tag); PINV(cpf); }
                u32x4 fr[4];
#pragma unroll
                for (int k = 0; k < 4; ++k) fr[k] = ldstr2(la + e * 4096 + k * 32, lb + e * 4096 + k * 32);
#pragma unroll
                for (int k = 0; k < 4; ++k) {
                    if (!gu && g4 == 3) { fr[k][2] = 0; fr[k][3] = 0; }
                    stc(fr[k], rWs, e == 3 ? l16 + off4 : l16, dst + (4 * e + k) * dA);
                }
            }
        }
        LBAR();
    } else {
        const unsigned i = i1;
        const unsigned r = w & 3, q4 = i16 >> 2, p4 = i16 & 3, rt = 4 * i + r;
        constexpr unsigned MFB = 114688, RSB = MFB + 2048, CSB = RSB + 512;
        const unsigned p0 = w < 4 ? (g == 9 ? 1 : 0) : (g == 9 ? 0 : 2);
        const bool isv = 3 * g + p0 >= 28;
        f32x16 a0 = {}, a1 = {};
        unsigned xa[4], wa[4], xb[4], wb[4];
        auto rdaddr = [&]() AI {
#pragma unroll
            for (int s = 0; s < 4; ++s) {
                const unsigned sp = (s + r) & 3;
                xa[s] = LB + (32 * r + t32) * 128 + (((2 * sp + hh) ^ ((t32 >> 1) & 7)) << 4);
                wa[s] = LB + 16384 + (16 * sp + 8 * hh + q4) * 192 + p0 * 64 + (4 * (g4 & 1) + p4) * 8;
                xb[s] = xa[s] + 2 * SLOT; wb[s] = wa[s] + 2 * SLOT;
                PINV(xb[s]); PINV(wb[s]);
            }
        };
        auto xad = [&](int c, int s) AI { return ((c & 2) ? xb[s] : xa[s]) + (c & 1) * SLOT; };
        auto wad = [&](int c, int s) AI { return ((c & 2) ? wb[s] : wa[s]) + (c & 1) * SLOT; };
        struct Dst { unsigned va, vb, vx, so; bool sa, sb; };
        auto mkdst = [&](unsigned T) AI {
            Dst d; const unsigned part = T / 14, tp = T % 14;
            if (T >= 28) { const unsigned c = 32 * tp + t32, hd = c / 28; d.va = hd * 262144 + (c - 28 * hd + 32 * hh) * 16; d.vb = d.va + 1024; d.vx = OOB; d.sa = d.sb = false; d.so = WS_KV + rt * 4096 + 2048; }
            else {
                const unsigned hs = part ? 262144 : 131072;
                const unsigned ca = 4 * tp + hh, pa = ca / 7, ra = ca - 7 * pa, cb = ca + 2, pb = cb / 7, rb = cb - 7 * pb;
                const unsigned ga = ra > 3 ? ra - 3 : ra, gb = rb > 3 ? rb - 3 : rb;
                d.va = (2 * pa + (ra > 3)) * hs + (ga >> 1) * 1024 + (t32 + 32 * (ga & 1)) * 16;
                d.vb = (2 * pb + (rb > 3)) * hs + (gb >> 1) * 1024 + (t32 + 32 * (gb & 1)) * 16;
                d.sa = ra == 3; d.sb = rb == 3;
                d.vx = ((2 * pb + 1) * hs + t32 * 16) | ((d.sa | d.sb) ? 0u : OOB);
                d.so = part ? WS_KV + rt * 4096 : WS_QF + rt * 2048;
            }
            PINV(d.va); PINV(d.vb); PINV(d.vx); PINS(d.so);
            return d;
        };
        auto fin = [&](auto V, f32x16 a, float rstd, const f32x4* rs, unsigned T, const Dst& d) AI {
            if (V) {
#pragma unroll
                for (int e = 0; e < 16; ++e) a[e] *= rs[e >> 2][e & 3];
                stc(pack8(a, 0), rWs, d.va, d.so); stc(pack8(a, 1), rWs, d.vb, d.so);
            } else {
                const float sc = rstd * (T < 14 ? QSCALE : 1.f);
#pragma unroll
                for (int e = 0; e < 16; ++e) a[e] *= sc;
                u32x4 v[2];
#pragma unroll
                for (int b = 0; b < 2; ++b) {
                    const u32x4 t = pack8(a, b);
                    const auto s0 = __builtin_amdgcn_permlane32_swap(t[0], t[2], false, false), s1 = __builtin_amdgcn_permlane32_swap(t[1], t[3], false, false);
                    v[b] = (u32x4){s0[0], s1[0], s0[1], s1[1]};
                }
                const unsigned pad = T < 14 ? 0u : 0x3f80u;
                const u32x4 vx = {pad, 0u, d.sa ? v[0][2] : v[1][2], d.sa ? v[0][3] : v[1][3]};
                if (d.sa) { v[0][2] = pad; v[0][3] = 0u; }
                if (d.sb) { v[1][2] = pad; v[1][3] = 0u; }
                stc(v[0], rWs, d.va, d.so); stc(v[1], rWs, d.vb, d.so); stc(vx, rWs, d.vx, d.so);
            }
        };
        rdaddr();
        unsigned kc = kc0 == 6 ? 0 : kc0 + 1;
        if (w >= 4) {
            __builtin_amdgcn_s_setprio(1);
            dma4v(rX, vx0, vx1, vx2, vx3, kc * 128, LB + SLOT + r * 4096);
            dma3v(rW, vw[0], vw[1], vw[2], kc * 64 * 2688, LB + SLOT + 16384 + r * 3072);
        }
        FENCE();
        if (w >= 4) {
            auto bodyB = [&](auto V) AI {
                f32x4 ca = {}, cb = {};
                u32x4 xf[4], w0[4];
                auto rd = [&](int c, int s) AI { xf[s] = ldsr128(xad(c, s)); w0[s] = ldstr2(wad(c, s), wad(c, s) + 768); };
                auto mm = [&](int s) AI { if (V) a0 = MFMA32(xf[s], w0[s], a0); else a0 = MFMA32(w0[s], xf[s], a0); };
                static_for<7>([&](auto CC) AI {
                    constexpr int c = CC;
                    kc = kc == 6 ? 0 : kc + 1;
                    if (c < 6) VBAR(7); else VBAR(0);
                    SBAR();
                    if (c > 0) { msum(w0[0], ca, cb); SBAR(); mm(0); SBAR(); }
                    rd(c, 0); SBAR();
                    if (c > 0) { mm(1); SBAR(); }
                    rd(c, 1); SBAR();
                    if (c > 0 && c < 5) { dma4v(rX, vx0, vx1, vx2, vx3, kc * 128, LB + ((c + 2) & 3) * SLOT + r * 4096); SBAR(); }
                    if (c > 0) { mm(2); SBAR(); }
                    rd(c, 2); SBAR();
                    if (c > 0) { mm(3); SBAR(); }
                    rd(c, 3); SBAR();
                    if (c == 0) dma4v(rX, vx0, vx1, vx2, vx3, kc * 128, LB + 2 * SLOT + r * 4096);
                    if (c < 5) { dma3v(rW, vw[0], vw[1], vw[2], kc * 64 * 2688, LB + ((c + 2) & 3) * SLOT + 16384 + r * 3072); SBAR(); }
                });
                __builtin_amdgcn_s_setprio(0);
                msum(w0[0], ca, cb); SBAR();
                mm(0); mm(1); mm(2); mm(3); SBAR();
                tag += TAG_STEP; PINV(tag);
                ldsw64(LB + CSB + p0 * 1024 + t32 * 32 + r * 8, split_c(swapadd(ca[0] + cb[0])));
                const Dst d0 = mkdst(3 * g + p0);
                unsigned cra = LB + CSB + p0 * 1024 + t32 * 32 + hh * 16, mra = LB + MFB + (32 * r + t32) * 16, rra = LB + RSB + (32 * r + 4 * hh) * 4;
                PINV(cra); PINV(mra); PINV(rra);
                LBAR();
                const u32x4 c0 = ldsr128(cra), mr = ldsr128(mra);
                f32x4 rs[4];
                if (V) {
#pragma unroll
                    for (int e = 0; e < 4; ++e) rs[e] = ldsrf4(rra + 32 * e);
                }
                const u32x4 mf = {mr[0], mr[1], mr[0], mr[1]};
                if (V) a0 = MFMA32(mf, c0, a0); else a0 = MFMA32(c0, mf, a0);
                fin(V, a0, __uint_as_float(mr[2]), rs, 3 * g + p0, d0);
            };
            if (isv) bodyB(std::true_type{}); else bodyB(std::false_type{});
        } else {
            unsigned s1b = WS_S1 + (i * 14 + 7 * (xcd & 1)) * 2048, s1w = hh ? OOB : kc0 * 2048 + (32 * r + t32) * 16;
            PINS(s1b); PINV(s1w);
            const Dst d0 = mkdst(3 * g + p0), d1 = mkdst(3 * g + p0 + 1);
            if (g >= 9) {
                const unsigned f = (i16 & 3) == 0 ? 0x3f803f80u : 0u, pvo = g4 * 262144 + (i16 >> 3) * 1024 + (28 + (i16 & 3) + 32 * ((i16 >> 2) & 1)) * 16;
#pragma unroll
                for (int p = 0; p < 3; ++p) {
                    const unsigned tp = 3 * g + p - 28, hd = 32 * tp / 28;
                    const unsigned nh = 3 * g + p < 28 ? 0 : 32 * tp - 28 * hd >= 24 ? 2 : 1;
                    stc((u32x4){f, f, f, f}, rWs, g4 < nh ? pvo : OOB, WS_KV + rt * 4096 + 2048 + (hd & 15) * 262144);
                }
            }
            unsigned cwa = LB + CSB + p0 * 1024 + t32 * 32 + r * 8, cra = LB + CSB + p0 * 1024 + t32 * 32 + hh * 16, mwa = LB + MFB + (32 * r + t32) * 16, rra = LB + RSB + (32 * r + 4 * hh) * 4, rwa = LB + RSB + (32 * r + t32) * 4;
            unsigned s1r = hh * 2048 + (32 * r + t32) * 16;
            PINV(cwa); PINV(cra); PINV(mwa); PINV(rra); PINV(rwa); PINV(s1r);
            tag += TAG_STEP; PINV(tag);
            const unsigned tl = (unsigned)tag;
            auto bodyA = [&](auto V) AI {
                f32x4 ca = {}, cb = {}, cc = {}, cd = {};
                u32x4 xf[4], w0[4], w1[4], sg[4];
                auto rd = [&](int s) AI { xf[s] = ldsr128(xa[s]); w0[s] = ldstr2(wa[s], wa[s] + 768); w1[s] = ldstr2(wa[s] + 64, wa[s] + 64 + 768); };
                auto mm = [&](int s) AI { if (V) { a0 = MFMA32(xf[s], w0[s], a0); a1 = MFMA32(xf[s], w1[s], a1); } else { a0 = MFMA32(w0[s], xf[s], a0); a1 = MFMA32(w1[s], xf[s], a1); } };
                BAR();
                rd(0); rd(1); rd(2); rd(3); SBAR();
                {
                    f32x4 sa = {}, sc = {}, qa = {}, qb = {};
#pragma unroll
                    for (int s = 0; s < 4; ++s) { msum(xf[s], sa, sc); msq(xf[s], qa, qb); }
                    qa += qb;
                    const float s1 = swapadd(sa[0] + sc[0]), s2 = swapadd((lane & 2) ? ((lane & 1) ? qa[3] : qa[2]) : ((lane & 1) ? qa[1] : qa[0]));
                    stc((u32x4){__float_as_uint(s1), tl, __float_as_uint(s2), tl}, rWs, s1w, s1b);
                }
                static_for<6>([&](auto CC) AI {
                    constexpr int c = 1 + CC;
                    BAR(); SBAR();
                    if (c == 6) { sg[0] = ldc(rWs, s1r, s1b); sg[1] = ldc(rWs, s1r, s1b + 4096); sg[2] = ldc(rWs, s1r, s1b + 8192); sg[3] = ldc(rWs, s1r, s1b + 10240); SBAR(); }
                    msum(w0[0], ca, cb); msum(w1[0], cc, cd); SBAR();
#pragma unroll
                    for (int s = 0; s < 4; ++s) {
                        if (V) a0 = MFMA32(xf[s], w0[s], a0); else a0 = MFMA32(w0[s], xf[s], a0);
                        SBAR();
                        w0[s] = ldstr2(wad(c, s), wad(c, s) + 768); SBAR();
                        if (V) a1 = MFMA32(xf[s], w1[s], a1); else a1 = MFMA32(w1[s], xf[s], a1);
                        SBAR();
                        xf[s] = ldsr128(xad(c, s)); w1[s] = ldstr2(wad(c, s) + 64, wad(c, s) + 64 + 768); SBAR();
                    }
                });
                msum(w0[0], ca, cb); msum(w1[0], cc, cd); SBAR();
                mm(0); mm(1); SBAR();
                __builtin_amdgcn_s_waitcnt(0x0F70);
#pragma nounroll
                for (unsigned spin = 0; spin < SPIN_CAP; ++spin) {
                    const bool ok = sg[0][1] == tl && sg[0][3] == tl && sg[1][1] == tl && sg[1][3] == tl && sg[2][1] == tl && sg[2][3] == tl && sg[3][1] == tl && sg[3][3] == tl;
                    if (__all(ok)) break;
                    FENCE();
                    sg[0] = ldc(rWs, s1r, s1b); sg[1] = ldc(rWs, s1r, s1b + 4096); sg[2] = ldc(rWs, s1r, s1b + 8192); sg[3] = ldc(rWs, s1r, s1b + 10240);
                    __builtin_amdgcn_s_waitcnt(0x0F70);
                }
                mm(2); mm(3); SBAR();
                const float s1 = swapadd(__uint_as_float(sg[0][0]) + __uint_as_float(sg[1][0]) + __uint_as_float(sg[2][0]) + (hh ? __uint_as_float(sg[3][0]) : 0.f));
                const float s2 = swapadd(__uint_as_float(sg[0][2]) + __uint_as_float(sg[1][2]) + __uint_as_float(sg[2][2]) + (hh ? __uint_as_float(sg[3][2]) : 0.f));
                const float mean = s1 * (1.f / 448.f), var = fmaxf(s2 * (1.f / 448.f) - mean * mean, 0.f), rstd = __builtin_amdgcn_rsqf(var + LN_EPS);
                const u32x2 m2 = split_m(mean);
                ldsw128(mwa, (u32x4){m2[0], m2[1], __float_as_uint(rstd), 0u}); ldswf(rwa, rstd);
                ldsw64(cwa, split_c(swapadd(ca[0] + cb[0]))); ldsw64(cwa + 1024, split_c(swapadd(cc[0] + cd[0])));
                const u32x4 mf = {m2[0], m2[1], m2[0], m2[1]};
                LBAR();
                const u32x4 c0 = ldsr128(cra), c1 = ldsr128(cra + 1024);
                f32x4 rs[4];
                if (V) {
#pragma unroll
                    for (int e = 0; e < 4; ++e) rs[e] = ldsrf4(rra + 32 * e);
                }
                if (V) { a0 = MFMA32(mf, c0, a0); a1 = MFMA32(mf, c1, a1); } else { a0 = MFMA32(c0, mf, a0); a1 = MFMA32(c1, mf, a1); }
                SBAR();
                fin(V, a0, rstd, rs, 3 * g + p0, d0);
                fin(V, a1, rstd, rs, 3 * g + p0 + 1, d1);
            };
            if (isv) bodyA(std::true_type{}); else bodyA(std::false_type{});
        }
        DRAINBAR();
        if (w == 7) publish(rWs, pv, WS_FA + (g * 16 + i) * 128, tag);
    }
    PINS(w);
    const unsigned wvo = w < 7 ? l16 : OOB;
    const unsigned j16 = (g4 >> 1) * 1024 + (i16 + 32 * (g4 & 1)) * 16, wvj = w < 7 ? j16 : OOB;
    u32x4 W[28], wf[16];
    u32x2 xr0, xr1;
    const unsigned rt32 = 4 * i3 + (j3 >> 2), ct3 = 7 * (j3 & 3) + min(w, 6u), rot3 = j3 >> 2;
    auto woload = [&](int e) AI { wf[e] = ldc(rWs, wvo + (e & 3) * 1024, WS_WO + ct3 * 16384 + (((e >> 2) + rot3) & 3) * 4096); };
    {
        const unsigned h = 2 * xcd + (bq >> 4), i = bq & 15;
        const unsigned u = w & 3, kh = w >> 2;
        unsigned kvs = WS_KV + (h * 64 + 32 * kh + u) * 4096;
        unsigned qs = WS_QF + (h * 64 + 4 * i) * 2048, w4b = WS_W4 + (7 * j4 + min(w, 6u)) * 28 * 1024;
        constexpr unsigned QB = 3 * 32768;
        unsigned rb = LB + kh * 16384 + l16, qa = LB + QB + u * 2048 + l16;
        auto w4load = [&](int p) AI { W[p] = ldc(rWs, wvj + (p & 1) * 256 + (p & 2) * 1024, w4b + (p >> 2) * 4096); };
        unsigned dv = bid < 196 ? (64 * (bid % 28) + 16 * (w >> 1) + (lane >> 2)) * 896 + (bid / 28) * 128 + (w & 1) * 64 + (lane & 3) * 16 : OOB;
        u32x4 dw;
        const unsigned cq = (16 * (i & 1) + i16 + 28) & 31, cx = i >> 1;
        unsigned fav = w != 7 ? OOB : (hh == 0 ? 224 + t32 : cq < 28 ? (7 * (cx & 1) + cq % 7) * 16 + 4 * (cx >> 1) + cq / 7 : 224 + (cq - 28) * 8 + cx) * 128;
        PINV(fav);
        constexpr unsigned EB = 3 * 32768;
        unsigned xw = LB + EB + w * 4096 + l16, ovo = (t32 >> 4) * 16384 + (i16 + 16 * hh) * 16, oso = WS_OF + ((8 * i + 2 * u) * 16 + h) * 1024 + kh * 512, fbo = WS_FB + i * 2048;
        unsigned dso = WS_WD + ((2 * (bid / 28) + (w & 1)) * 112 + 4 * (bid % 28) + (w >> 1)) * 1024;
        PINV(xw); PINV(ovo); PINS(oso); PINS(fbo); PINS(dso);
        PINS(kvs); PINS(qs); PINS(w4b); PINV(rb); PINV(qa); PINV(dv);
        if (w == 7) {
            waitf(rWs, ((14 + 14 * hh + (28 * h + 27 * (g4 & 1)) / 32) / 3 * 16 + i16) * 128, WS_FA, tag);
#pragma unroll
            for (int e = 0; e < 8; ++e) {
                dma4c(rWs, l16, kvs - (35 - 32 * (e & 1) - (e >> 1)) * 4096, LB + (e & 1) * 16384 + (e >> 1) * 4096);
            }
            VBAR(24);
        }
        if (w == 6) {
            waitf(rWs, ((28 * h + 27 * (lane & 1)) / 32 / 3 * 16 + i) * 128, WS_FA, tag);
            dma4c(rWs, l16, qs, LB + QB); dma4c(rWs, l16, qs + 4096, LB + QB + 4096);
            asm volatile("s_waitcnt vmcnt(0)" ::: "memory");
        }
        if (w != 7) BAR();
        u32x4 q0 = ldsr128(qa), q1 = ldsr128(qa + 1024);
        unsigned rb2 = rb + 65536, rb4 = rb + 131072;
        PINV(rb2); PINV(rb4);
        auto ta = [&](int t) AI { const int sl = (t >> 2) % 5, off = (t & 3) * 4096; return sl < 2 ? rb + sl * 32768 + off : sl < 4 ? rb2 + (sl - 2) * 32768 + off : rb4 + off; };
        f32x16 o = {}, S[2];
        u32x4 kf0, kf1, vf0, vf1, p0, p1 = {0u, 0u, 0u, 0u};
        u32x2 fa;
        float e14 = 0.f, e15 = 0.f;
        const f32x16 z16 = {};
        {
            kf0 = ldsr128(LB + l16); kf1 = ldsr128(LB + l16 + 1024);
            const u32x4 ka = ldsr128(ta(0)), kb = ldsr128(ta(0) + 1024);
            SBAR();
            f32x16 sl = MFMA32(kf0, q0, z16); sl = MFMA32(kf1, q1, sl);
            SBAR();
            dma4c(rWs, l16, kvs + 16384, LB + 32768 + w * 4096);
            vf0 = ldsr128(ta(0) + 2048); vf1 = vf0;
            float m = fmaxf(fmaxf(sl[0], sl[1]), sl[2]), m2 = fmaxf(fmaxf(sl[3], sl[4]), sl[5]);
#pragma unroll
            for (int e = 6; e < 14; e += 4) { m = fmaxf(fmaxf(m, sl[e]), sl[e + 1]); m2 = fmaxf(fmaxf(m2, sl[e + 2]), sl[e + 3]); }
            m = swapmax(fmaxf(fmaxf(fmaxf(m, sl[14]), sl[15]), m2));
            const unsigned nm = pk2(-m, 0.f);
            q0[0] = (unsigned)lane < (h & 1) * 32 ? nm : q0[0]; q1[2] = (unsigned)lane >= 32 + (h & 1) * 32 ? nm : q1[2];
            PINV(q0); PINV(q1);
            SBAR();
            VBAR(4);
            kf0 = ldsr128(ta(1)); kf1 = ldsr128(ta(1) + 1024);
            S[0] = MFMA32(ka, q0, z16); S[0] = MFMA32(kb, q1, S[0]);
            SBAR();
            if (b1 >= 28 && w == 7) publish(rWs, pv, WS_FA + (224 + (b1 - 28) * 8 + xcd) * 128, tag);
        }
#define EX(v) __builtin_amdgcn_exp2f(v)
        auto nD = [](int t) constexpr { return t < 4 || (t >= 6 && t < 10) || (t >= 14 && t < 18) ? 2 : 0; };
        auto nW = [](int t) constexpr { return t < 6 || t > 29 ? 0 : t >= 10 && t < 14 ? 2 : 1; };
        auto cW = [nW](int t) constexpr { int c = 0; for (int k = 0; k < t; ++k) c += nW(k); return c; };
        static_assert(cW(32) == 28, "all gate/up fragments are fetched");
        static_for<32>([&](auto TT) AI {
            constexpr int t = TT;
            f32x16& SC = S[t & 1]; f32x16& SN = S[(t + 1) & 1];
            if (t == 2) VBAR(4);
            if (t == 6) {
                __builtin_amdgcn_s_waitcnt(0x0F70);
                asm volatile("s_waitcnt vmcnt(0)" : "+v"(fa) :: "memory");
                if (w == 7 && !__all(fa[0] == (unsigned)tag && fa[1] == (unsigned)(tag >> 32))) waitf(rWs, fav, WS_FA, tag);
                BAR();
            }
            if (t == 14) VBARN(cW(14) - cW(9) + 1);
            if (t == 22) VBARN(cW(22) - cW(17));
            __builtin_amdgcn_s_setprio(((t >= 22 ? 31 : t >= 14 ? 21 : t >= 6 ? 13 : t >= 2 ? 5 : 1) - t) & 3);
            float a0_, a1_, a2_, a3_;
            u32x4 nk0, nk1, nv0, nv1;
            __builtin_amdgcn_s_waitcnt(0xC07F);
            SBAR();
            if (t < 31) SN = MFMA32(kf0, q0, z16);
            SBAR();
            nv1 = ldsr128(ta(t) + 3072);
            if (t < 30) nk0 = ldsr128(ta(t + 2));
            SBAR();
            a0_ = EX(SC[0]); a1_ = EX(SC[1]); SBAR(); p1[3] = pk2(e14, e15); SBAR(); a2_ = EX(SC[2]); a3_ = EX(SC[3]); SBAR(); p0[0] = pk2(a0_, a1_); SBAR();
            if (t > 0) o = MFMA32(vf1, p1, o);
            SBAR();
            if (t < 30) nk1 = ldsr128(ta(t + 2) + 1024);
            if (t < 31) nv0 = ldsr128(ta(t + 1) + 2048);
            SBAR();
            a0_ = EX(SC[4]); a1_ = EX(SC[5]); SBAR(); p0[1] = pk2(a2_, a3_); SBAR(); a2_ = EX(SC[6]); a3_ = EX(SC[7]); SBAR(); p0[2] = pk2(a0_, a1_); SBAR();
            if (t < 31) SN = MFMA32(kf1, q1, SN);
            SBAR();
            if constexpr (nD(t) > 0) { constexpr int stg = t < 4 ? 2 + (t >> 1) : t < 10 ? 1 + (t >> 1) : (t >> 1) - 1; dma2c(rWs, l16, kvs + stg * 16384 + (t & 1) * 2048, LB + (stg % 5) * 32768 + w * 4096 + (t & 1) * 2048); }
            if constexpr (nW(t) == 2) w4load(cW(t) + 1);
            if constexpr (t >= 23 && t < 27) woload(t - 23);
            if constexpr (t == 29 || t == 30) woload(4 + 2 * (t - 29));
            if constexpr (t == 27) xr0 = *(const GAS u32x2*)(x + (32 * rt32 + i16) * 896 + ct3 * 32 + g4 * 8);
            if constexpr (t == 28) xr1 = *(const GAS u32x2*)(x + (32 * rt32 + 16 + i16) * 896 + ct3 * 32 + g4 * 8);
            SBAR();
            a0_ = EX(SC[8]); a1_ = EX(SC[9]); SBAR(); p0[3] = pk2(a2_, a3_); SBAR(); a2_ = EX(SC[10]); a3_ = EX(SC[11]); SBAR(); p1[0] = pk2(a0_, a1_); SBAR();
            o = MFMA32(vf0, p0, o); SBAR();
            if constexpr (nW(t) > 0) w4load(cW(t));
            if constexpr (t == 29 || t == 30) woload(5 + 2 * (t - 29));
            if constexpr (t == 9) dw = __builtin_amdgcn_raw_buffer_load_b128(__builtin_amdgcn_make_buffer_rsrc((void*)wd, 0, 1792 * 896, 0x00020000), dv, 0, 0);
            if constexpr (t == 22) ldsw128(xw + 3072, dw);
            if constexpr (t == 23) { const unsigned a = LB + EB + 3072 + w * 4096 + (8 * hh + (i16 >> 2)) * 64 + (g4 & 1) * 32 + (i16 & 3) * 8; dw = ldstr2(a, a + 256); }
            if constexpr (t == 24) stc(dw, rWs, bid < 196 ? l16 : OOB, dso);
            if (t == 4) asm volatile("buffer_load_dwordx2 %0, %1, %2, %3 offen sc1" : "=&v"(fa) : "v"(fav), "s"(rWs), "s"(WS_FA) : "memory");
            SBAR();
            a0_ = EX(SC[12]); a1_ = EX(SC[13]); SBAR(); p1[1] = pk2(a2_, a3_); SBAR(); e14 = EX(SC[14]); e15 = EX(SC[15]); SBAR(); p1[2] = pk2(a0_, a1_); SBAR();
            if (t == 5) { PINV(e14); PINV(e15); PINV(p1); }
            vf1 = nv1;
            if (t < 30) { kf0 = nk0; kf1 = nk1; }
            if (t < 31) vf0 = nv0;
        });
        __builtin_amdgcn_s_setprio(0);
        p1[3] = pk2(e14, e15); o = MFMA32(vf1, p1, o);
        {
            f32x4 ka, kb, sa4, sb4;
            if (kh) { sa4 = (f32x4){o[0], o[1], o[2], o[3]}; sb4 = (f32x4){o[4], o[5], o[6], o[7]}; ka = (f32x4){o[8], o[9], o[10], o[11]}; kb = (f32x4){o[12], o[13], o[14], o[15]}; }
            else { ka = (f32x4){o[0], o[1], o[2], o[3]}; kb = (f32x4){o[4], o[5], o[6], o[7]}; sa4 = (f32x4){o[8], o[9], o[10], o[11]}; sb4 = (f32x4){o[12], o[13], o[14], o[15]}; }
            ldswf4(xw, sa4); ldswf4(xw + 1024, sb4); ldswf(xw + 2048, o[12]);
            const unsigned xr = xw ^ 16384;
            LBAR();
            ka += ldsrf4(xr); kb += ldsrf4(xr + 1024);
            float l = hi2all(o[12] + ldsrf(xr + 2048));
            const float am = ((fabsf(ka[0]) + fabsf(ka[1])) + (fabsf(ka[2]) + fabsf(ka[3]))) + ((fabsf(kb[0]) + fabsf(kb[1])) + (fabsf(kb[2]) + fabsf(kb[3])));
            const bool good = l >= 0x1p-40f && l <= 0x1p60f && am <= 0x1p120f;
            if (__builtin_expect(!__all(good), 0)) {
                const unsigned kvb = WS_KV + h * 64 * 4096;
                float mx = -INFINITY;
#pragma nounroll
                for (unsigned t = 0; t < 64; ++t) {
                    f32x16 s = MFMA32(ldc(rWs, l16, kvb + t * 4096), q0, z16); s = MFMA32(ldc(rWs, l16, kvb + t * 4096 + 1024), q1, s);
#pragma unroll
                    for (int e = 0; e < 16; ++e) mx = fmaxf(mx, s[e]);
                }
                mx = swapmax(mx);
                o = z16;
#pragma nounroll
                for (unsigned t = 0; t < 64; ++t) {
                    f32x16 s = MFMA32(ldc(rWs, l16, kvb + t * 4096), q0, z16); s = MFMA32(ldc(rWs, l16, kvb + t * 4096 + 1024), q1, s);
#pragma unroll
                    for (int e = 0; e < 16; ++e) s[e] = __builtin_amdgcn_exp2f(s[e] - mx);
                    o = MFMA32(ldc(rWs, l16, kvb + t * 4096 + 2048), pack8(s, 0), o); o = MFMA32(ldc(rWs, l16, kvb + t * 4096 + 3072), pack8(s, 1), o);
                }
                l = hi2all(o[12]);
                if (kh) { ka = (f32x4){o[8], o[9], o[10], o[11]}; kb = (f32x4){o[12], o[13], o[14], o[15]}; }
                else { ka = (f32x4){o[0], o[1], o[2], o[3]}; kb = (f32x4){o[4], o[5], o[6], o[7]}; }
            }
            const float rl = __builtin_amdgcn_rcpf(l);
            u32x4 of = {pk2(ka[0] * rl, ka[1] * rl), pk2(ka[2] * rl, ka[3] * rl), pk2(kb[0] * rl, kb[1] * rl), pk2(kb[2] * rl, kb[3] * rl)};
            if (kh && hh) { of[2] = 0; of[3] = 0; }
            stc(of, rWs, ovo, oso);
        }
        if (w == 7) __builtin_amdgcn_s_setprio(3);
        WAITBAR(0);
        if (w == 7) { arrive16(rWs, i16 * 128, fbo); publish(rWs, pv, WS_FW + bid * 8, tag); }
    }
    const unsigned av = i16 * 128;
    u32x4 Wd[28];
    const unsigned m5 = j3 / 7, n5 = j3 % 7;
    const unsigned gs0 = WS_GF + (4 * i3 + m5) * 114688;
    const unsigned rot5 = (n5 + m5 + 2 * (i3 & 1)) % 3;
    auto gko = [&](int j) AI { unsigned c = j + rot5; c = c >= 3 ? c - 3 : c; return j == 3 ? 24576u : c * 8192; };
    const unsigned wdb = WS_WD + ((2 * n5 + (w & 1)) * 112 + 28 * (w >> 1)) * 1024, wdv = j3 < 14 ? j16 : OOB;
    auto wdload = [&](int p) AI { Wd[p] = ldc(rWs, wdv + (p & 1) * 256 + (p & 2) * 1024, wdb + gko(p >> 3) + ((p >> 2) & 1) * 4096); };
    {
        constexpr unsigned PSB = 122880;
        constexpr unsigned SB = 114688;
        if (w == 7) {
            {
            waitc(rWs, WS_FB + (i3 * 16 + j3) * 128);
#pragma unroll
            for (int e = 0; e < 8; ++e) {
                dma4c(rWs, l16, WS_OF + rt32 * 32768 + (e & 1) * 16384 + (((e >> 1) + rot3) & 3) * 4096, LB + (e & 1) * 16384 + (e >> 1) * 4096);
                if (e == 5) VBAR(16);
            }
            VBAR(16);
            u32x4 fw0 = ldc(rWs, l16, WS_FW), fw1 = ldc(rWs, l16 + 1024, WS_FW);
            VBAR(2);
            __builtin_amdgcn_s_waitcnt(0x0F70);
            const unsigned tl = (unsigned)tag, th = (unsigned)(tag >> 32);
#pragma nounroll
            for (unsigned spin = 0; spin < SPIN_CAP; ++spin) {
                if (__all(fw0[0] == tl && fw0[1] == th && fw0[2] == tl && fw0[3] == th && fw1[0] == tl && fw1[1] == th && fw1[2] == tl && fw1[3] == th)) break;
                FENCE();
                fw0 = ldc(rWs, l16, WS_FW); fw1 = ldc(rWs, l16 + 1024, WS_FW);
                __builtin_amdgcn_s_waitcnt(0x0F70);
            }
#pragma unroll
            for (int p = 0; p < 28; ++p) wdload(p);
            BAR();
            arrive16(rWs, av, WS_FC + i3 * 2048);
            }
            unsigned hb = WS_HF + 4 * i4 * 28672;
            PINS(hb);
            waitc(rWs, WS_FC + (i4 * 16 + j4) * 128);
            auto chunk = [&](int ps, unsigned c) AI { dma4c(rWs, l16, hb + 2 * ps * 28672 + c * 4096, LB + 2 * ps * 28672 + c * 4096); dma4c(rWs, l16, hb + (2 * ps + 1) * 28672 + c * 4096, LB + (2 * ps + 1) * 28672 + c * 4096); };
            chunk(0, 0); chunk(0, 1);
            unsigned c = 2 + j4 % 5;
            chunk(0, c); c = c == 6 ? 2 : c + 1; chunk(0, c);
            VBAR(16);
#pragma unroll
            for (int k = 0; k < 3; ++k) { c = c == 6 ? 2 : c + 1; chunk(0, c); }
            chunk(1, 0);
            VBAR(8);
            __builtin_amdgcn_s_setprio(0);
            chunk(1, 1);
            u32x4 sv[8];
            const unsigned tl = (unsigned)tag;
#pragma nounroll
            for (unsigned spin = 0; spin < SPIN_CAP; ++spin) {
                bool ok = true;
                FENCE();
#pragma unroll
                for (int e = 0; e < 8; ++e) sv[e] = ldc(rWs, l16 + (e & 3) * 1024, WS_ST + i4 * 8192 + (e >> 2) * 4096);
#pragma unroll
                for (int e = 0; e < 8; ++e) ok &= (sv[e][1] == tl) & (sv[e][3] == tl);
                if (__all(ok)) break;
            }
            __builtin_amdgcn_s_waitcnt(0x0F70);
#pragma unroll
            for (int e = 0; e < 2; ++e)
                ldsw64(LB + SB + e * 512 + lane * 8, (u32x2){__float_as_uint((__uint_as_float(sv[e][0]) + __uint_as_float(sv[e + 2][0])) + (__uint_as_float(sv[e + 4][0]) + __uint_as_float(sv[e + 6][0]))),
                                                            __float_as_uint((__uint_as_float(sv[e][2]) + __uint_as_float(sv[e + 2][2])) + (__uint_as_float(sv[e + 4][2]) + __uint_as_float(sv[e + 6][2])))});
#pragma unroll
            for (int e = 0; e < 2; ++e) {
                const unsigned a = LB + SB + e * 512 + ((lane >> 4) * 32 + i16) * 4;
                const float mean = ldsrf(a) * (1.f / 448.f), var = fmaxf(ldsrf(a + 64) * (1.f / 448.f) - mean * mean, 0.f);
                const u32x2 m2 = split_m(mean);
                ldsw128(LB + SB + 1024 + e * 1024 + l16, (u32x4){m2[0], m2[1], __float_as_uint(__builtin_amdgcn_rcpf(var + LN_EPS)), 0u});
            }
#pragma unroll
            for (int k = 0; k < 3; ++k) { c = c == 6 ? 2 : c + 1; chunk(1, c); }
            LBAR();
#pragma unroll
            for (int k = 0; k < 2; ++k) { c = c == 6 ? 2 : c + 1; chunk(1, c); }
            VBAR(0);
            arrive16(rWs, av, WS_FD + i4 * 2048);
            if (j3 < 14) {
                waitc(rWs, WS_FD + (i3 * 16 + j3) * 128);
#pragma unroll
                for (int e = 0; e < 16; ++e) dma4c(rWs, l16, gs0 + ((e >> 1) & 3) * 28672 + gko(e >> 3) + (e & 1) * 4096, LB + (e < 8 ? 131072 : 0) + (e & 7) * 4096);
            }
            __builtin_amdgcn_s_setprio(3);
        } else {
            u32x2 cg, cu;
            {
#pragma unroll
            for (int e = 8; e < 16; ++e) woload(e);
            {
                f32x4 ca = {}, cb = {}, cc = {}, cd = {};
#pragma unroll
                for (int p = 0; p < 28; p += 2) { msum(W[p], ca, cb); msum(W[p + 1], cc, cd); }
                const float sg = swapadd(ca[0] + cb[0]), su = swapadd(cc[0] + cd[0]);
                const auto rg = __builtin_amdgcn_permlane16_swap(__float_as_uint(sg), __float_as_uint(sg), false, false), ru = __builtin_amdgcn_permlane16_swap(__float_as_uint(su), __float_as_uint(su), false, false);
                cg = split_c(__uint_as_float(rg[0]) + __uint_as_float(rg[1])); cu = split_c(__uint_as_float(ru[0]) + __uint_as_float(ru[1]));
                if (g4) { cg = (u32x2){0u, 0u}; cu = cg; }
                PINV(cg); PINV(cu);
            }
            unsigned so = WS_HF + (rt32 * 28 + ct3) * 1024, sto = LB + PSB + w * 256 + lane * 4;
            f32x4 d0 = {bflo(xr0[0]), bfhi(xr0[0]), bflo(xr0[1]), bfhi(xr0[1])}, d1 = {bflo(xr1[0]), bfhi(xr1[0]), bflo(xr1[1]), bfhi(xr1[1])};
            PINS(so); PINV(sto); PINV(d0); PINV(d1);
            DRAINBAR();
            u32x4 of[8];
#pragma unroll
            for (int e = 0; e < 4; ++e) { of[2 * e] = ldsr128(LB + e * 1024 + l16); of[2 * e + 1] = ldsr128(LB + 16384 + e * 1024 + l16); }
            SBAR();
#pragma unroll
            for (int e = 0; e < 16; ++e) {
                d0 = MFMA16(wf[e], of[2 * (e & 3)], d0); d1 = MFMA16(wf[e], of[2 * (e & 3) + 1], d1);
                SBAR();
                if (e == 0 || e == 4) BAR();
                if (e < 12) { of[2 * (e & 3)] = ldsr128(LB + (e + 4) * 1024 + l16); of[2 * (e & 3) + 1] = ldsr128(LB + 16384 + (e + 4) * 1024 + l16); }
                SBAR();
            }
            const u32x2 h0 = {pk2(d0[0], d0[1]), pk2(d0[2], d0[3])}, h1 = {pk2(d1[0], d1[1]), pk2(d1[2], d1[3])};
            {
                const auto s0 = __builtin_amdgcn_permlane16_swap(h0[0], h1[0], false, false), s1 = __builtin_amdgcn_permlane16_swap(h0[1], h1[1], false, false);
                stc((u32x4){s0[0], s1[0], s0[1], s1[1]}, rWs, l16, so);
            }
            const unsigned a0 = h0[0], a1 = h0[1], b0 = h1[0], b1 = h1[1];
            const float sA = dot2(a0, 0x3f803f80u, dot2(a1, 0x3f803f80u, 0.f)), sB = dot2(a0, a0, dot2(a1, a1, 0.f)), sC = dot2(b0, 0x3f803f80u, dot2(b1, 0x3f803f80u, 0.f)), sD = dot2(b0, b0, dot2(b1, b1, 0.f));
            const auto rx = __builtin_amdgcn_permlane32_swap(__float_as_uint(sA), __float_as_uint(sC), false, false);
            const auto ry = __builtin_amdgcn_permlane32_swap(__float_as_uint(sB), __float_as_uint(sD), false, false);
            const auto rz = __builtin_amdgcn_permlane16_swap(__float_as_uint(__uint_as_float(rx[0]) + __uint_as_float(rx[1])), __float_as_uint(__uint_as_float(ry[0]) + __uint_as_float(ry[1])), false, false);
            ldswf(sto, __uint_as_float(rz[0]) + __uint_as_float(rz[1]));
            DRAINBAR();
            if (w == 6) {
                float t = 0.f;
#pragma unroll
                for (int e = 0; e < 7; ++e) t += ldsrf(LB + PSB + e * 256 + lane * 4);
                __builtin_amdgcn_raw_buffer_store_b64((u32x2){__float_as_uint(t), (unsigned)tag}, rWs, lane * 8, WS_ST + i3 * 8192 + (j3 & 3) * 2048 + (j3 >> 2) * 512, SC1);
            }
            }
            unsigned ha = LB + j16, gso = WS_GF + (4 * i4 * 112 + 7 * j4 + w) * 1024, sa = LB + SB + 1024 + i16 * 16;
            PINV(ha); PINS(gso); PINV(sa);
            u32x4 hf[3][2];
            BAR();
            hf[0][0] = ldsr128(ha); hf[0][1] = ldsr128(ha + 256); hf[1][0] = ldsr128(ha + 28672); hf[1][1] = ldsr128(ha + 28672 + 256);
            static_for<2>([&](auto PS) AI {
                constexpr int ps = PS;
                f32x4 ag[4] = {}, au[4] = {};
                u32x4 mr[4];
                static_for<28>([&](auto PP) AI {
                    constexpr int p = PP, q = 28 * ps + p;
                    if (ps == 0 && (p == 6 || p == 25)) BAR();
                    if (ps == 1 && p == 6) { __builtin_amdgcn_s_waitcnt(0x0F70); VBAR(0); }
                    if (q == 26) { ha += 57344; PINV(ha); }
                    if constexpr (p >= 6 && p < 26 && (p - 6) % 5 == 0) __builtin_amdgcn_s_setprio(3 - (p - 6) / 5);
#pragma unroll
                    for (int a = 0; a < 2; ++a) {
                        ag[2 * (p & 1) + a] = MFMA16(W[p & ~1], hf[q % 3][a], ag[2 * (p & 1) + a]);
                        au[2 * (p & 1) + a] = MFMA16(W[p | 1], hf[q % 3][a], au[2 * (p & 1) + a]);
                        if (q + 2 < 56) hf[(q + 2) % 3][a] = ldsr128(ha + (p & 1) * 28672 + (((q + 2) % 28) >> 1) * 2048 + a * 256);
                        SBAR();
                    }
                    if constexpr (p >= 26) { mr[2 * p - 52] = ldsr128(sa + (4 * ps + 2 * p - 52) * 256); mr[2 * p - 51] = ldsr128(sa + (4 * ps + 2 * p - 51) * 256); }
                    if constexpr (ps == 0 && (p & 1) == 0) wdload(p / 2);
                    if constexpr (ps == 1 && p >= 6 && p < 20) wdload(8 + p);
                    SBAR();
                });
#pragma unroll
                for (int a = 0; a < 4; ++a) { const u32x2 m2 = {mr[a][0], mr[a][1]}; ag[a] = MFMA16K(cg, m2, ag[a]); au[a] = MFMA16K(cu, m2, au[a]); }
                __builtin_amdgcn_s_setprio(3);
#pragma unroll
                for (int b = 0; b < 2; ++b) {
                    unsigned pk[4];
#pragma unroll
                    for (int k = 0; k < 4; ++k) {
                        const int a = 2 * b + (k >> 1), e = 2 * (k & 1);
                        float t0, t1;
                        asm("v_max_f32 %0, 0, %1" : "=v"(t0) : "v"(ag[a][e] * __uint_as_float(mr[a][2])));
                        asm("v_max_f32 %0, 0, %1" : "=v"(t1) : "v"(ag[a][e + 1] * __uint_as_float(mr[a][2])));
                        t0 *= au[a][e]; t1 *= au[a][e + 1]; asm("" : "+v"(t0)); asm("" : "+v"(t1));
                        pk[k] = pk2(t0, t1);
                    }
                    const auto s0 = __builtin_amdgcn_permlane16_swap(pk[0], pk[2], false, false), s1 = __builtin_amdgcn_permlane16_swap(pk[1], pk[3], false, false);
                    stc((u32x4){s0[0], s1[0], s0[1], s1[1]}, rWs, l16, gso + (2 * ps + b) * 114688);
                }
                __builtin_amdgcn_s_setprio(0);
                SBAR();
            });
        }
    }
    const unsigned kq = w >> 1, row = tid >> 3, c8 = tid & 7;
    unsigned gs = gs0 + kq * 28672, ga = LB + kq * 8192 + j16, gb = ga + 65536, gc = ga + 131072, fdo = WS_FD + 32768 + i4 * 2048, fdw = WS_FD + 32768 + (i3 * 16 + j3) * 128;
    PINS(gs); PINV(ga); PINV(gb); PINV(gc); PINS(fdo); PINS(fdw);
    DRAINBAR();
    if (w == 7) arrive16(rWs, av, fdo);
    if (j3 < 14) {
        auto slot = [](int j) constexpr { return j % 5 == 0 ? 131072 : (j % 5 - 1) * 32768; };
        auto stage = [&](int j) AI {
            const unsigned src = gs + (j >> 2) * 2 * 114688 + gko(j & 3);
            if ((j & 3) < 3) dma4c(rWs, l16, src + (w & 1) * 4096, LB + slot(j) + w * 4096);
            else dma2c(rWs, l16, src + (w & 1) * 2048, LB + slot(j) + kq * 8192 + (w & 1) * 2048);
        };
        f32x4 acc[2][4] = {};
        u32x4 gf[8];
        auto gadr = [&](int j) AI { return slot(j) < 65536 ? ga + slot(j) : slot(j) < 131072 ? gb + (slot(j) - 65536) : gc; };
        auto rdg = [&](int j, int e) AI { gf[e] = ldsr128(gadr(j) + (e >> 1) * 2048 + (e & 1) * 256); };
        auto mmg = [&](int j, int e) AI {
            f32x4* a = acc[j >> 2] + 2 * (e & 1);
            a[0] = MFMA16(Wd[8 * (j & 3) + (e & ~1)], gf[e], a[0]); a[1] = MFMA16(Wd[8 * (j & 3) + (e | 1)], gf[e], a[1]);
        };
#pragma unroll
        for (int e = 0; e < 8; ++e) rdg(0, e);
        SBAR();
        stage(2);
        unsigned rw[2], ra, rb, ovo, oso, hso = WS_HF + ((4 * i3 + m5) * 28 + 4 * n5) * 1024;
        u32x4 hres;
        PINS(hso);
        auto spillA = [&]() AI {
#pragma unroll
            for (int e = 0; e < 4; ++e) ldswf4(rw[e & 1] + (e >> 1) * 4096, acc[0][e]);
        };
        static_for<8>([&](auto JJ) AI {
            constexpr int j = 1 + JJ, n = (j & 3) < 3 ? 8 : 4, pn = ((j - 1) & 3) < 3 ? 8 : 4;
            if (j == 2) {
                static_for<8>([&](auto E) AI { mmg(1, E); });
                SBAR();
                if (w == 7) waitc(rWs, fdw);
                __builtin_amdgcn_s_setprio(0);
#pragma unroll
                for (int e = 0; e < 2; ++e) { rw[e] = LB + 65536 + kq * 8192 + i16 * 256 + (((8 * (w & 1) + 4 * e + g4) ^ i16) << 4); PINV(rw[e]); }
                WAITBAR(0);
                stage(4);
            }
            if (j == 4) { static_for<4>([&](auto E) AI { mmg(3, E); }); SBAR(); WAITBAR(4); }
            if (j == 5) { static_for<8>([&](auto E) AI { mmg(4, E); SBAR(); if (E == 4) stage(7); SBAR(); }); WAITBAR(6); }
            if (j == 6) { if (w >= 4) spillA(); static_for<8>([&](auto E) AI { mmg(5, E); }); SBAR(); if (w < 4) spillA(); VBAR(3); }
            if (j == 7) { static_for<4>([&](auto E) AI { mmg(6, E); }); SBAR(); VBAR(1); }
            SBAR();
#pragma unroll
            for (int e = 0; e < 8; ++e) {
                if ((j == 1 || j == 3 || j == 8 || (j == 7 && e >= 4)) && e < pn) mmg(j - 1, e);
                SBAR();
                if (j < 8 && e < n) rdg(j, e);
                if (j == 1 && e == 3) stage(3);
                if (j == 5 && e == 5) asm volatile("buffer_load_dwordx4 %0, %1, %2, %3 offen sc1" : "=&v"(hres) : "v"((tid >> 8) * 57344 + (c8 >> 1) * 1024 + ((row & 31) + 32 * (c8 & 1)) * 16),
                                                   "s"(rWs), "s"(hso) : "memory");
                if (j == 3 && e == 5) stage(5);
                if (j == 4 && e == 7) stage(6);
                SBAR();
            }
            if (j == 4) {
                ra = LB + 65536 + (row >> 5) * 32768 + (row & 31) * 256 + (((2 * c8) ^ (row & 15)) << 4); rb = ra ^ 16;
                ovo = ((row >> 5) * 64 + (row & 31)) * 896 + c8 * 16; oso = (128 * i3 + 32 * m5) * 896 + n5 * 128;
                PINV(ra); PINV(rb); PINV(ovo); PINS(oso);
            }
        });
#pragma unroll
        for (int e = 0; e < 4; ++e) ldswf4(rw[e & 1] + (e >> 1) * 4096 + 32768, acc[1][e]);
        asm volatile("s_waitcnt vmcnt(0)" : "+v"(hres) :: "memory");
        f32x4 lo = {bflo(hres[0]), bfhi(hres[0]), bflo(hres[1]), bfhi(hres[1])}, hi = {bflo(hres[2]), bfhi(hres[2]), bflo(hres[3]), bfhi(hres[3])};
        PINV(lo); PINV(hi);
        LBAR();
        f32x4 pl[4], ph[4];
#pragma unroll
        for (int k = 0; k < 4; ++k) { pl[k] = ldsrf4(ra + k * 8192); ph[k] = ldsrf4(rb + k * 8192); }
        lo += (pl[0] + pl[1]) + (pl[2] + pl[3]); hi += (ph[0] + ph[1]) + (ph[2] + ph[3]);
        stc((u32x4){pk2(lo[0], lo[1]), pk2(lo[2], lo[3]), pk2(hi[0], hi[1]), pk2(hi[2], hi[3])}, __builtin_amdgcn_make_buffer_rsrc(out, 0, 2048 * 896, 0x00020000), ovo, oso);
    } else if (bid == 255 && w == 7) {
#pragma unroll
        for (int k = 0; k < 4; ++k) waitf(rWs, lane * 128, WS_FA + k * 8192, tag);
        publish(rWs, pv, WS_E, tag);
    }
}

extern "C" void kernel_launch(void* const* d_in, const int* in_sizes, int n_in, void* d_out, int out_size, void* d_ws, size_t ws_size, hipStream_t stream) {
    block_fwd<<<256, 512, 0, stream>>>((const char*)d_in[0], (const char*)d_in[1], (const char*)d_in[2], (const char*)d_in[4], (const char*)d_in[5], (const char*)d_in[6],
                                       (char*)d_out, (char*)d_ws);
}
```
